```python
import torch

def _softmax(x, axis=-1):
    return torch.softmax(x, dim=axis)

def _gelu(x):
    return torch.nn.functional.gelu(x, approximate='tanh')

def _silu(x):
    return torch.nn.functional.silu(x)

def _sigmoid(x):
    return torch.sigmoid(x)

def _layernorm(x, axis=-1, eps=1e-05):
    m = x.mean(dim=axis, keepdim=True)
    v = x.var(dim=axis, keepdim=True, unbiased=False)
    return (x - m) / torch.sqrt(v + eps)

def _scatter_add(tgt, idx, src, axis=0):
    return tgt.clone().index_add_(axis, idx, src)

def _segment_sum(src, seg, n):
    out = torch.zeros((n,) + src.shape[1:], dtype=src.dtype)
    return out.index_add_(0, seg, src)

def _pad(x, pw):
    flat = tuple((v for pair in reversed(pw) for v in pair))
    return torch.nn.functional.pad(x, flat)
kernel_name = 'adaLN_cond__mha_block_cached__swiglu_ffn__decode'

def setup_inputs():

    _w = lambda a: (lambda t: t.to(torch.bfloat16) if t.is_floating_point() else t)(torch.as_tensor(a))

    def _A_setup():
        B, Sq, D = (1, 1, 640)
        H, Sk, Dh = (32, 32768, 192)
        return {'x': _w((lambda *s: torch.randn(*s, dtype=torch.bfloat16) * 0.1)(B, Sq, D)), 'k_cache': _w((lambda *s: torch.randn(*s, dtype=torch.bfloat16) * 0.8)(B, H, Sk, Dh)), 'v_cache': _w((lambda *s: torch.randn(*s, dtype=torch.bfloat16) * 0.8)(B, H, Sk, Dh)), 'wq': _w((lambda *s: torch.randn(*s, dtype=torch.bfloat16) * 0.8)(D, H * Dh) * D ** (-0.5)), 'wo': _w((lambda *s: torch.randn(*s, dtype=torch.bfloat16) * 0.8)(H * Dh, D) * (H * Dh) ** (-0.5))}
    _A_in = _A_setup()

    def _B_setup():
        B, S, D, F = (1, 1, 640, 1536)
        return {'x': _w((lambda *s: torch.randn(*s, dtype=torch.bfloat16) * 0.1)(B, S, D)), 'w_gate': _w((lambda *s: torch.randn(*s, dtype=torch.bfloat16) * 0.1)(D, F)), 'w_up': _w((lambda *s: torch.randn(*s, dtype=torch.bfloat16) * 0.1)(D, F)), 'w_down': _w((lambda *s: torch.randn(*s, dtype=torch.bfloat16) * 0.1)(F, D))}
    _B_in = _B_setup()
    return {'A_x': _A_in['x'], 'A_k_cache': _A_in['k_cache'], 'A_v_cache': _A_in['v_cache'], 'A_wq': _A_in['wq'], 'A_wo': _A_in['wo'], 'B_x': _B_in['x'], 'B_w_gate': _B_in['w_gate'], 'B_w_up': _B_in['w_up'], 'B_w_down': _B_in['w_down'], 'c': _w((lambda *s: torch.randn(*s, dtype=torch.bfloat16) * 0.8)(1, 640) * 8.0), 'w_mod': _w((lambda *s: torch.randn(*s, dtype=torch.bfloat16) * 0.8)(640, 6 * 640) * 640 ** (-0.5) * 8.0)}

def reference(A_x, A_k_cache, A_v_cache, A_wq, A_wo, B_x, B_w_gate, B_w_up, B_w_down, c, w_mod):

    def _A_ref(x, k_cache, v_cache, wq, wo):
        B, Sq, D = x.shape
        H, Dh = (32, 192)
        q = (lambda x, *d: x.permute(*d))((x @ wq).reshape(B, Sq, H, Dh), 0, 2, 1, 3)
        scores = torch.einsum('bhqd,bhkd->bhqk', q, k_cache) * Dh ** (-0.5)
        p = _softmax(scores, axis=-1)
        o = torch.einsum('bhqk,bhkd->bhqd', p, v_cache)
        return (lambda x, *d: x.permute(*d))(o, 0, 2, 1, 3).reshape(B, Sq, H * Dh) @ wo

    def _B_ref(x, w_gate, w_up, w_down):
        g = _silu(x @ w_gate)
        u = x @ w_up
        return g * u @ w_down
    x = A_x
    mod = (_silu(c) @ w_mod).reshape(c.shape[0], 6, 1, -1)
    sa, ca, ga, sb, cb, gb = (mod[:, 0].clone(), mod[:, 1].clone(), mod[:, 2].clone(), mod[:, 3].clone(), mod[:, 4].clone(), mod[:, 5].clone())
    h = _layernorm(x, axis=-1) * (1 + ca) + sa
    x = x + ga * _A_ref(h, A_k_cache, A_v_cache, A_wq, A_wo)
    h = _layernorm(x, axis=-1) * (1 + cb) + sb
    x = x + gb * _B_ref(h, B_w_gate, B_w_up, B_w_down)
    return x
```

```cpp
#include <hip/hip_runtime.h>
#include <hip/hip_bf16.h>
#include <cstdint>

#define MIMIC 1

typedef unsigned short u16;
using u32x4 = __attribute__((ext_vector_type(4))) unsigned;
using bf16x2 = __attribute__((ext_vector_type(2))) __bf16;

constexpr int D = 640, H = 32, SK = 32768, DH = 192, HD = H * DH, F = 1536, NMOD = 6 * D;
constexpr int NSPLIT = 16;

__device__ __forceinline__ float bf2f(u16 v) { return __builtin_bit_cast(float, (unsigned)v << 16); }
__device__ __forceinline__ float rbf(float x) {
  return (float)(__bf16)x;
}
__device__ __forceinline__ float rbf_always(float x) { return (float)(__bf16)x; }
__device__ __forceinline__ float lo(unsigned u) { return __builtin_bit_cast(float, u << 16); }
__device__ __forceinline__ float hi(unsigned u) { return __builtin_bit_cast(float, u & 0xffff0000u); }
__device__ __forceinline__ float dot2(unsigned k, unsigned q, float acc) {
  return __builtin_amdgcn_fdot2_f32_bf16(__builtin_bit_cast(bf16x2, k), __builtin_bit_cast(bf16x2, q), acc, false);
}
template <int CTRL> __device__ __forceinline__ float dpp(float x) {
  return __builtin_bit_cast(float, __builtin_amdgcn_mov_dpp(__builtin_bit_cast(int, x), CTRL, 0xf, 0xf, true));
}
constexpr int XOR1 = 0xB1, XOR2 = 0x4E, HMIR = 0x141;

__device__ __forceinline__ float silu(float x) { return x / (1.f + __expf(-x)); }

__device__ float block_sum(float v, float* red) {
  for (int o = 32; o > 0; o >>= 1) v += __shfl_xor(v, o);
  const int w = threadIdx.x >> 6, nw = blockDim.x >> 6;
  __syncthreads();
  if ((threadIdx.x & 63) == 0) red[w] = v;
  __syncthreads();
  float s = 0.f;
  for (int i = 0; i < nw; ++i) s += red[i];
  return s;
}

__device__ void gemv64(const float* a, int K, const u16* W, int N, int n0, float* red  , float* out64) {
  const int tid = threadIdx.x, c = tid & 7, r = tid >> 3;
  float acc[8];
#pragma unroll
  for (int i = 0; i < 8; ++i) acc[i] = 0.f;
  for (int k = r; k < K; k += 32) {
    u32x4 w = *reinterpret_cast<const u32x4*>(W + (size_t)k * N + n0 + c * 8);
    float av = a[k];
#pragma unroll
    for (int i = 0; i < 4; ++i) { acc[2 * i] = fmaf(av, lo(w[i]), acc[2 * i]); acc[2 * i + 1] = fmaf(av, hi(w[i]), acc[2 * i + 1]); }
  }
  __syncthreads();
#pragma unroll
  for (int i = 0; i < 8; ++i) red[r * 64 + c * 8 + i] = acc[i];
  __syncthreads();
  if (tid < 64) { float s = 0.f; for (int i = 0; i < 32; ++i) s += red[i * 64 + tid]; out64[tid] = s; }
  __syncthreads();
}

__device__ void ln_mod(const float* x, const float* sh, const float* sc, float* h, float* red) {
  const int tid = threadIdx.x;
  float s = 0.f;
  for (int i = tid; i < D; i += blockDim.x) s += x[i];
  const float mean = block_sum(s, red) * (1.f / D);
  float q = 0.f;
  for (int i = tid; i < D; i += blockDim.x) { float d = x[i] - mean; q += d * d; }
  const float var = block_sum(q, red) * (1.f / D);
  const float mb = rbf(mean), vb = rbf(var);
  const float den = rbf(sqrtf(rbf(vb + 1e-5f)));
  for (int i = tid; i < D; i += blockDim.x) {
    float n = rbf(rbf(x[i] - mb) / den);
    h[i] = rbf(rbf(n * rbf(1.f + sc[i])) + sh[i]);
  }
  __syncthreads();
}

__global__ void __launch_bounds__(256) k_mod(const u16* c, const u16* wmod, float* mod) {
  __shared__ float a[D]; __shared__ float red[32 * 64]; __shared__ float o[64];
  for (int i = threadIdx.x; i < D; i += 256) a[i] = rbf(silu(bf2f(c[i])));
  __syncthreads();
  gemv64(a, D, wmod, NMOD, blockIdx.x * 64, red, o);
  if (threadIdx.x < 64) mod[blockIdx.x * 64 + threadIdx.x] = rbf(o[threadIdx.x]);
}

__global__ void __launch_bounds__(256) k_q(const u16* x, const float* mod, const u16* wq, float* q) {
  __shared__ float xs[D]; __shared__ float h[D]; __shared__ float red[32 * 64]; __shared__ float o[64];
  for (int i = threadIdx.x; i < D; i += 256) xs[i] = bf2f(x[i]);
  __syncthreads();
  ln_mod(xs, mod, mod + D, h, red);
  gemv64(h, D, wq, HD, blockIdx.x * 64, red, o);
  if (threadIdx.x < 64) q[blockIdx.x * 64 + threadIdx.x] = rbf_always(o[threadIdx.x]);
}

__global__ void __launch_bounds__(256) k_attn(const float* q, const u16* K, const u16* V, float* part) {
  __shared__ float mrg[4][DH + 2];
  const int s = blockIdx.x, h = blockIdx.y;
  const int tid = threadIdx.x, lane = tid & 63, wave = tid >> 6, r = lane >> 3, c = lane & 7;
  unsigned qp[12];
#pragma unroll
  for (int j = 0; j < 3; ++j)
#pragma unroll
    for (int i = 0; i < 4; ++i) {
      float a = q[h * DH + j * 64 + c * 8 + 2 * i], b = q[h * DH + j * 64 + c * 8 + 2 * i + 1];
      qp[j * 4 + i] = (__builtin_bit_cast(unsigned, a) >> 16) | (__builtin_bit_cast(unsigned, b) & 0xffff0000u);
    }
  constexpr int KPS = SK / NSPLIT;
  const size_t base = ((size_t)h * SK + (size_t)s * KPS) * DH;
  float m = -INFINITY, l = 0.f, acc[24];
#pragma unroll
  for (int i = 0; i < 24; ++i) acc[i] = 0.f;
  const float scale = 0.07216878364870322f;
  for (int g = wave; g < KPS / 8; g += 4) {
    const u16* kp = K + base + (size_t)(g * 8 + r) * DH + c * 8;
    const u16* vp = V + base + (size_t)(g * 8 + r) * DH + c * 8;
    u32x4 kk[3], vv[3];
#pragma unroll
    for (int j = 0; j < 3; ++j) kk[j] = __builtin_nontemporal_load(reinterpret_cast<const u32x4*>(kp + j * 64));
#pragma unroll
    for (int j = 0; j < 3; ++j) vv[j] = __builtin_nontemporal_load(reinterpret_cast<const u32x4*>(vp + j * 64));
    float d = 0.f;
#pragma unroll
    for (int j = 0; j < 3; ++j)
#pragma unroll
      for (int i = 0; i < 4; ++i) d = dot2(kk[j][i], qp[j * 4 + i], d);
    d += dpp<XOR1>(d); d += dpp<XOR2>(d); d += dpp<HMIR>(d);
    const float sc = rbf(rbf(d) * scale);
    const float mn = fmaxf(m, sc);
    const float alpha = __expf(m - mn), p = __expf(sc - mn);
    l = l * alpha + p; m = mn;
#pragma unroll
    for (int j = 0; j < 3; ++j)
#pragma unroll
      for (int i = 0; i < 4; ++i) {
        acc[j * 8 + 2 * i] = fmaf(p, lo(vv[j][i]), acc[j * 8 + 2 * i] * alpha);
        acc[j * 8 + 2 * i + 1] = fmaf(p, hi(vv[j][i]), acc[j * 8 + 2 * i + 1] * alpha);
      }
  }
  float M = m;
  for (int o = 8; o < 64; o <<= 1) M = fmaxf(M, __shfl_xor(M, o));
  const float w = __expf(m - M);
  l *= w;
  for (int o = 8; o < 64; o <<= 1) l += __shfl_xor(l, o);
#pragma unroll
  for (int i = 0; i < 24; ++i) { float a = acc[i] * w; for (int o = 8; o < 64; o <<= 1) a += __shfl_xor(a, o); acc[i] = a; }
  if (r == 0) {
#pragma unroll
    for (int j = 0; j < 3; ++j)
#pragma unroll
      for (int i = 0; i < 8; ++i) mrg[wave][2 + j * 64 + c * 8 + i] = acc[j * 8 + i];
    if (c == 0) { mrg[wave][0] = M; mrg[wave][1] = l; }
  }
  __syncthreads();
  if (tid < DH) {
    float MM = fmaxf(fmaxf(mrg[0][0], mrg[1][0]), fmaxf(mrg[2][0], mrg[3][0]));
    float L = 0.f, o = 0.f;
    for (int w2 = 0; w2 < 4; ++w2) { float e = __expf(mrg[w2][0] - MM); L += e * mrg[w2][1]; o += e * mrg[w2][2 + tid]; }
    float* pp = part + (size_t)(h * NSPLIT + s) * (DH + 2);
    pp[2 + tid] = o;
    if (tid == 0) { pp[0] = MM; pp[1] = L; }
  }
}

__global__ void __launch_bounds__(192) k_comb(const float* part, float* o) {
  const int h = blockIdx.x, d = threadIdx.x;
  const float* pp = part + (size_t)h * NSPLIT * (DH + 2);
  float M = -INFINITY;
  for (int s = 0; s < NSPLIT; ++s) M = fmaxf(M, pp[s * (DH + 2)]);
  float L = 0.f, a = 0.f;
  for (int s = 0; s < NSPLIT; ++s) { float e = __expf(pp[s * (DH + 2)] - M); L += e * pp[s * (DH + 2) + 1]; a += e * pp[s * (DH + 2) + 2 + d]; }
  o[h * DH + d] = rbf(a / L);
}

__global__ void __launch_bounds__(256) k_wo(const float* o, const u16* wo, float* ypart) {
  __shared__ float a[768]; __shared__ float red[32 * 64]; __shared__ float out[64];
  const int rc = blockIdx.y;
  for (int i = threadIdx.x; i < 768; i += 256) a[i] = o[rc * 768 + i];
  __syncthreads();
  gemv64(a, 768, wo + (size_t)rc * 768 * D, D, blockIdx.x * 64, red, out);
  if (threadIdx.x < 64) ypart[rc * D + blockIdx.x * 64 + threadIdx.x] = out[threadIdx.x];
}

__global__ void __launch_bounds__(256) k_gu(const u16* x, const float* mod, const float* ypart, const u16* wg, const u16* wu, float* act, float* x1out) {
  __shared__ float xs[D]; __shared__ float h[D]; __shared__ float red[32 * 64]; __shared__ float g[64]; __shared__ float u[64];
  for (int i = threadIdx.x; i < D; i += 256) {
    float y = 0.f;
    for (int rc = 0; rc < 8; ++rc) y += ypart[rc * D + i];
    xs[i] = rbf(bf2f(x[i]) + rbf(mod[2 * D + i] * rbf(y)));
    if (blockIdx.x == 0) x1out[i] = xs[i];
  }
  __syncthreads();
  ln_mod(xs, mod + 3 * D, mod + 4 * D, h, red);
  gemv64(h, D, wg, F, blockIdx.x * 64, red, g);
  gemv64(h, D, wu, F, blockIdx.x * 64, red, u);
  if (threadIdx.x < 64) act[blockIdx.x * 64 + threadIdx.x] = rbf(rbf(silu(rbf(g[threadIdx.x]))) * rbf(u[threadIdx.x]));
}

__global__ void __launch_bounds__(256) k_down(const float* act, const float* mod, const float* x1, const u16* wd, __hip_bfloat16* out) {
  __shared__ float a[F]; __shared__ float red[32 * 64]; __shared__ float o[64];
  for (int i = threadIdx.x; i < F; i += 256) a[i] = act[i];
  __syncthreads();
  gemv64(a, F, wd, D, blockIdx.x * 64, red, o);
  if (threadIdx.x < 64) {
    const int n = blockIdx.x * 64 + threadIdx.x;
    float v = x1[n] + rbf(mod[5 * D + n] * rbf(o[threadIdx.x]));
    reinterpret_cast<__bf16*>(out)[n] = (__bf16)v;
  }
}

extern "C" void kernel_launch(void* const* d_in, const int* in_sizes, int n_in, void* d_out, int out_size, void* d_ws, size_t ws_size,
                              hipStream_t stream) {
  const u16* x = (const u16*)d_in[0];
  const u16* K = (const u16*)d_in[1];
  const u16* V = (const u16*)d_in[2];
  const u16* wq = (const u16*)d_in[3];
  const u16* wo = (const u16*)d_in[4];
  const u16* wg = (const u16*)d_in[6];
  const u16* wu = (const u16*)d_in[7];
  const u16* wd = (const u16*)d_in[8];
  const u16* c = (const u16*)d_in[9];
  const u16* wmod = (const u16*)d_in[10];
  float* ws = (float*)d_ws;
  float* mod = ws;
  float* q = ws + 4096;
  float* o = ws + 4096 + 8192;
  float* ypart = ws + 32768;
  float* act = ws + 49152;
  float* x1 = ws + 53248;
  float* part = ws + 65536;
  k_mod<<<NMOD / 64, 256, 0, stream>>>(c, wmod, mod);
  k_q<<<HD / 64, 256, 0, stream>>>(x, mod, wq, q);
  k_attn<<<dim3(NSPLIT, H), 256, 0, stream>>>(q, K, V, part);
  k_comb<<<H, 192, 0, stream>>>(part, o);
  k_wo<<<dim3(10, 8), 256, 0, stream>>>(o, wo, ypart);
  k_gu<<<F / 64, 256, 0, stream>>>(x, mod, ypart, wg, wu, act, x1);
  k_down<<<D / 64, 256, 0, stream>>>(act, mod, x1, wd, (__hip_bfloat16*)d_out);
}
```

```cpp
#define WNT 1
#define VLO 192
#define VHI 256
#include <hip/hip_runtime.h>
#include <hip/hip_bf16.h>
#include <cstdint>

#define MIMIC 1

typedef unsigned short u16;
using u32x4 = __attribute__((ext_vector_type(4))) unsigned;
using bf16x2 = __attribute__((ext_vector_type(2))) __bf16;

constexpr int D = 640, H = 32, SK = 32768, DH = 192, HD = H * DH, F = 1536, NMOD = 6 * D;
constexpr int NSPLIT = 8;

__device__ __forceinline__ float bf2f(u16 v) { return __builtin_bit_cast(float, (unsigned)v << 16); }
__device__ __forceinline__ float rbf(float x) {
  return (float)(__bf16)x;
}
__device__ __forceinline__ float rbf_always(float x) { return (float)(__bf16)x; }
__device__ __forceinline__ float lo(unsigned u) { return __builtin_bit_cast(float, u << 16); }
__device__ __forceinline__ float hi(unsigned u) { return __builtin_bit_cast(float, u & 0xffff0000u); }
__device__ __forceinline__ float dot2(unsigned k, unsigned q, float acc) {
  return __builtin_amdgcn_fdot2_f32_bf16(__builtin_bit_cast(bf16x2, k), __builtin_bit_cast(bf16x2, q), acc, false);
}
template <int CTRL> __device__ __forceinline__ float dpp(float x) {
  return __builtin_bit_cast(float, __builtin_amdgcn_mov_dpp(__builtin_bit_cast(int, x), CTRL, 0xf, 0xf, true));
}
constexpr int XOR1 = 0xB1, XOR2 = 0x4E, HMIR = 0x141;

__device__ __forceinline__ float silu(float x) { return x / (1.f + __expf(-x)); }

__device__ float block_sum(float v, float* red) {
  for (int o = 32; o > 0; o >>= 1) v += __shfl_xor(v, o);
  const int w = threadIdx.x >> 6, nw = blockDim.x >> 6;
  __syncthreads();
  if ((threadIdx.x & 63) == 0) red[w] = v;
  __syncthreads();
  float s = 0.f;
  for (int i = 0; i < nw; ++i) s += red[i];
  return s;
}

__device__ void gemv64(const float* a, int K, const u16* W, int N, int n0, float* red  , float* out64) {
  const int tid = threadIdx.x, c = tid & 7, r = tid >> 3;
  float acc[8];
#pragma unroll
  for (int i = 0; i < 8; ++i) acc[i] = 0.f;
  for (int k = r; k < K; k += 32) {
    u32x4 w = __builtin_nontemporal_load(reinterpret_cast<const u32x4*>(W + (size_t)k * N + n0 + c * 8));
    float av = a[k];
#pragma unroll
    for (int i = 0; i < 4; ++i) { acc[2 * i] = fmaf(av, lo(w[i]), acc[2 * i]); acc[2 * i + 1] = fmaf(av, hi(w[i]), acc[2 * i + 1]); }
  }
  __syncthreads();
#pragma unroll
  for (int i = 0; i < 8; ++i) red[r * 64 + c * 8 + i] = acc[i];
  __syncthreads();
  if (tid < 64) { float s = 0.f; for (int i = 0; i < 32; ++i) s += red[i * 64 + tid]; out64[tid] = s; }
  __syncthreads();
}

__device__ void ln_mod(const float* x, const float* sh, const float* sc, float* h, float* red) {
  const int tid = threadIdx.x;
  float s = 0.f;
  for (int i = tid; i < D; i += blockDim.x) s += x[i];
  const float mean = block_sum(s, red) * (1.f / D);
  float q = 0.f;
  for (int i = tid; i < D; i += blockDim.x) { float d = x[i] - mean; q += d * d; }
  const float var = block_sum(q, red) * (1.f / D);
  const float mb = rbf(mean), vb = rbf(var);
  const float den = rbf(sqrtf(rbf(vb + 1e-5f)));
  for (int i = tid; i < D; i += blockDim.x) {
    float n = rbf(rbf(x[i] - mb) / den);
    h[i] = rbf(rbf(n * rbf(1.f + sc[i])) + sh[i]);
  }
  __syncthreads();
}

__global__ void __launch_bounds__(256) k_mod(const u16* c, const u16* wmod, float* mod) {
  __shared__ float a[D]; __shared__ float red[32 * 64]; __shared__ float o[64];
  for (int i = threadIdx.x; i < D; i += 256) a[i] = rbf(silu(bf2f(c[i])));
  __syncthreads();
  gemv64(a, D, wmod, NMOD, blockIdx.x * 64, red, o);
  if (threadIdx.x < 64) mod[blockIdx.x * 64 + threadIdx.x] = rbf(o[threadIdx.x]);
}

__global__ void __launch_bounds__(256) k_q(const u16* x, const float* mod, const u16* wq, float* q) {
  __shared__ float xs[D]; __shared__ float h[D]; __shared__ float red[32 * 64]; __shared__ float o[64];
  for (int i = threadIdx.x; i < D; i += 256) xs[i] = bf2f(x[i]);
  __syncthreads();
  ln_mod(xs, mod, mod + D, h, red);
  gemv64(h, D, wq, HD, blockIdx.x * 64, red, o);
  if (threadIdx.x < 64) q[blockIdx.x * 64 + threadIdx.x] = rbf_always(o[threadIdx.x]);
}

#define KLO 256
#define KHI 256
constexpr size_t TB = (size_t)H * SK * DH * 2;
template <int AUXK, int AUXV>
__device__ __forceinline__ void attn_body(const u16* K, const u16* V, unsigned off, const unsigned (&qp)[12], float& m, float& l, float (&acc)[24]) {
  __amdgpu_buffer_rsrc_t rk = __builtin_amdgcn_make_buffer_rsrc((void*)K, 0, (int)TB, 0x00020000);
  __amdgpu_buffer_rsrc_t rv = __builtin_amdgcn_make_buffer_rsrc((void*)V, 0, (int)TB, 0x00020000);
  const float scale = 0.07216878364870322f;
  constexpr int NIT = (SK / NSPLIT) / 8 / 8;
  constexpr unsigned DSTEP = 8 * 3072;
  u32x4 kk[2][3], vv[2][3];
#define LD(b, o) do { _Pragma("unroll") for (int j = 0; j < 3; ++j) kk[b][j] = __builtin_amdgcn_raw_buffer_load_b128(rk, (o) + j * 128, 0, AUXK); \
    _Pragma("unroll") for (int j = 0; j < 3; ++j) vv[b][j] = __builtin_amdgcn_raw_buffer_load_b128(rv, (o) + j * 128, 0, AUXV); } while (0)
#define USE(b) do { float d = 0.f; \
    _Pragma("unroll") for (int j = 0; j < 3; ++j) _Pragma("unroll") for (int i = 0; i < 4; ++i) d = dot2(kk[b][j][i], qp[j * 4 + i], d); \
    d += dpp<XOR1>(d); d += dpp<XOR2>(d); d += dpp<HMIR>(d); \
    const float sc = rbf(rbf(d) * scale); const float mn = fmaxf(m, sc); \
    const float alpha = __expf(m - mn), p = __expf(sc - mn); l = l * alpha + p; m = mn; \
    _Pragma("unroll") for (int j = 0; j < 3; ++j) _Pragma("unroll") for (int i = 0; i < 4; ++i) { \
        acc[j * 8 + 2 * i] = fmaf(p, lo(vv[b][j][i]), acc[j * 8 + 2 * i] * alpha); \
        acc[j * 8 + 2 * i + 1] = fmaf(p, hi(vv[b][j][i]), acc[j * 8 + 2 * i + 1] * alpha); } } while (0)
  LD(0, off); off += DSTEP;
  for (int it = 0; it < NIT; it += 2) {
    LD(1, off); off += DSTEP;
    USE(0);
    if (it + 2 < NIT) { LD(0, off); off += DSTEP; }
    USE(1);
  }
}
__global__ void __launch_bounds__(512) k_attn(const float* q, const u16* K, const u16* V, float* part) {
  __shared__ float mrg[8][DH + 2];
  const int s = blockIdx.x % NSPLIT, h = blockIdx.x / NSPLIT;
  const int tid = threadIdx.x, lane = tid & 63, wave = __builtin_amdgcn_readfirstlane(tid >> 6), r = lane >> 3, c = lane & 7;
  unsigned qp[12];
#pragma unroll
  for (int j = 0; j < 3; ++j)
#pragma unroll
    for (int i = 0; i < 4; ++i) {
      float a = q[h * DH + j * 64 + c * 8 + 2 * i], b = q[h * DH + j * 64 + c * 8 + 2 * i + 1];
      qp[j * 4 + i] = (__builtin_bit_cast(unsigned, a) >> 16) | (__builtin_bit_cast(unsigned, b) & 0xffff0000u);
    }
  constexpr int KPS = SK / NSPLIT;
  const unsigned off = (unsigned)(((size_t)h * SK + (size_t)s * KPS + wave * 8 + r) * DH * 2 + c * 16);
  float m = -INFINITY, l = 0.f, acc[24];
#pragma unroll
  for (int i = 0; i < 24; ++i) acc[i] = 0.f;
  const int b = blockIdx.x;
  const bool kd = b >= KLO && b < KHI, vd = b >= VLO && b < VHI;
  if (kd && vd) attn_body<0, 0>(K, V, off, qp, m, l, acc);
  else if (kd) attn_body<0, 2>(K, V, off, qp, m, l, acc);
  else if (vd) attn_body<2, 0>(K, V, off, qp, m, l, acc);
  else attn_body<2, 2>(K, V, off, qp, m, l, acc);
  float M = m;
  for (int o = 8; o < 64; o <<= 1) M = fmaxf(M, __shfl_xor(M, o));
  const float w = __expf(m - M);
  l *= w;
  for (int o = 8; o < 64; o <<= 1) l += __shfl_xor(l, o);
#pragma unroll
  for (int i = 0; i < 24; ++i) { float a = acc[i] * w; for (int o = 8; o < 64; o <<= 1) a += __shfl_xor(a, o); acc[i] = a; }
  if (r == 0) {
#pragma unroll
    for (int j = 0; j < 3; ++j)
#pragma unroll
      for (int i = 0; i < 8; ++i) mrg[wave][2 + j * 64 + c * 8 + i] = acc[j * 8 + i];
    if (c == 0) { mrg[wave][0] = M; mrg[wave][1] = l; }
  }
  __syncthreads();
  if (tid < DH) {
    float MM = -INFINITY;
    for (int w2 = 0; w2 < 8; ++w2) MM = fmaxf(MM, mrg[w2][0]);
    float L = 0.f, o = 0.f;
    for (int w2 = 0; w2 < 8; ++w2) { float e = __expf(mrg[w2][0] - MM); L += e * mrg[w2][1]; o += e * mrg[w2][2 + tid]; }
    float* pp = part + (size_t)(h * NSPLIT + s) * (DH + 2);
    pp[2 + tid] = o;
    if (tid == 0) { pp[0] = MM; pp[1] = L; }
  }
}

__global__ void __launch_bounds__(192) k_comb(const float* part, float* o) {
  const int h = blockIdx.x, d = threadIdx.x;
  const float* pp = part + (size_t)h * NSPLIT * (DH + 2);
  float M = -INFINITY;
  for (int s = 0; s < NSPLIT; ++s) M = fmaxf(M, pp[s * (DH + 2)]);
  float L = 0.f, a = 0.f;
  for (int s = 0; s < NSPLIT; ++s) { float e = __expf(pp[s * (DH + 2)] - M); L += e * pp[s * (DH + 2) + 1]; a += e * pp[s * (DH + 2) + 2 + d]; }
  o[h * DH + d] = rbf(a / L);
}

__global__ void __launch_bounds__(256) k_wo(const float* o, const u16* wo, float* ypart) {
  __shared__ float a[768]; __shared__ float red[32 * 64]; __shared__ float out[64];
  const int rc = blockIdx.y;
  for (int i = threadIdx.x; i < 768; i += 256) a[i] = o[rc * 768 + i];
  __syncthreads();
  gemv64(a, 768, wo + (size_t)rc * 768 * D, D, blockIdx.x * 64, red, out);
  if (threadIdx.x < 64) ypart[rc * D + blockIdx.x * 64 + threadIdx.x] = out[threadIdx.x];
}

__global__ void __launch_bounds__(256) k_gu(const u16* x, const float* mod, const float* ypart, const u16* wg, const u16* wu, float* act, float* x1out) {
  __shared__ float xs[D]; __shared__ float h[D]; __shared__ float red[32 * 64]; __shared__ float g[64]; __shared__ float u[64];
  for (int i = threadIdx.x; i < D; i += 256) {
    float y = 0.f;
    for (int rc = 0; rc < 8; ++rc) y += ypart[rc * D + i];
    xs[i] = rbf(bf2f(x[i]) + rbf(mod[2 * D + i] * rbf(y)));
    if (blockIdx.x == 0) x1out[i] = xs[i];
  }
  __syncthreads();
  ln_mod(xs, mod + 3 * D, mod + 4 * D, h, red);
  gemv64(h, D, wg, F, blockIdx.x * 64, red, g);
  gemv64(h, D, wu, F, blockIdx.x * 64, red, u);
  if (threadIdx.x < 64) act[blockIdx.x * 64 + threadIdx.x] = rbf(rbf(silu(rbf(g[threadIdx.x]))) * rbf(u[threadIdx.x]));
}

__global__ void __launch_bounds__(256) k_down(const float* act, const float* mod, const float* x1, const u16* wd, __hip_bfloat16* out) {
  __shared__ float a[F]; __shared__ float red[32 * 64]; __shared__ float o[64];
  for (int i = threadIdx.x; i < F; i += 256) a[i] = act[i];
  __syncthreads();
  gemv64(a, F, wd, D, blockIdx.x * 64, red, o);
  if (threadIdx.x < 64) {
    const int n = blockIdx.x * 64 + threadIdx.x;
    float v = x1[n] + rbf(mod[5 * D + n] * rbf(o[threadIdx.x]));
    reinterpret_cast<__bf16*>(out)[n] = (__bf16)v;
  }
}

extern "C" void kernel_launch(void* const* d_in, const int* in_sizes, int n_in, void* d_out, int out_size, void* d_ws, size_t ws_size,
                              hipStream_t stream) {
  const u16* x = (const u16*)d_in[0];
  const u16* K = (const u16*)d_in[1];
  const u16* V = (const u16*)d_in[2];
  const u16* wq = (const u16*)d_in[3];
  const u16* wo = (const u16*)d_in[4];
  const u16* wg = (const u16*)d_in[6];
  const u16* wu = (const u16*)d_in[7];
  const u16* wd = (const u16*)d_in[8];
  const u16* c = (const u16*)d_in[9];
  const u16* wmod = (const u16*)d_in[10];
  float* ws = (float*)d_ws;
  float* mod = ws;
  float* q = ws + 4096;
  float* o = ws + 4096 + 8192;
  float* ypart = ws + 32768;
  float* act = ws + 49152;
  float* x1 = ws + 53248;
  float* part = ws + 65536;
  k_mod<<<NMOD / 64, 256, 0, stream>>>(c, wmod, mod);
  k_q<<<HD / 64, 256, 0, stream>>>(x, mod, wq, q);
  k_attn<<<NSPLIT * H, 512, 0, stream>>>(q, K, V, part);
  k_comb<<<H, 192, 0, stream>>>(part, o);
  k_wo<<<dim3(10, 8), 256, 0, stream>>>(o, wo, ypart);
  k_gu<<<F / 64, 256, 0, stream>>>(x, mod, ypart, wg, wu, act, x1);
  k_down<<<D / 64, 256, 0, stream>>>(act, mod, x1, wd, (__hip_bfloat16*)d_out);
}
```

```cpp
#include <hip/hip_runtime.h>
#include <cstdint>

typedef unsigned short u16;
typedef unsigned long long u64;
using u32x4 = __attribute__((ext_vector_type(4))) unsigned;
using u32x3 = __attribute__((ext_vector_type(3))) unsigned;
using u32x2 = __attribute__((ext_vector_type(2))) unsigned;
using bf16x2 = __attribute__((ext_vector_type(2))) __bf16;
typedef __amdgpu_buffer_rsrc_t rsrc_t;

#define NP 4
#define NL 6
#define PF_DEPTH 6
#define POLL_A 2
#define POLL_B 1
#define STAG 37

constexpr int D = 640, H = 32, SK = 32768, DH = 192, HD = H * DH, F = 1536, NMOD = 6 * D;
constexpr unsigned SENT = 0xFFFFFFFFu, OOB = 0x7ff00000u;
constexpr int NT = SK / 8;
constexpr int TILEB = 8 * DH * 2;
constexpr int NPS = (NP + NL) / 2;
constexpr int NSTAT = NPS * 64 + 2 * 128;
constexpr unsigned SPIN_CAP = 1u << 18;
constexpr unsigned W_EPOCH = 0, W_MODG = 4096, W_RDY = W_MODG + 1920 * 8, W_QS = 32768, W_ML = 49152, W_CNT = 57344, W_YP = 65536, W_ACT = 90112,
                   W_PART = 131072, W_USED = W_PART + 512 * DH * 4;
constexpr unsigned W_STAMP = 1u << 20;

__device__ __forceinline__ float bf2f(unsigned v) { return __builtin_bit_cast(float, v << 16); }
__device__ __forceinline__ float rbf(float x) { return (float)(__bf16)x; }
__device__ __forceinline__ float lo(unsigned u) { return __builtin_bit_cast(float, u << 16); }
__device__ __forceinline__ float hi(unsigned u) { return __builtin_bit_cast(float, u & 0xffff0000u); }
__device__ __forceinline__ unsigned f2u(float x) { return __builtin_bit_cast(unsigned, x); }
__device__ __forceinline__ float u2f(unsigned x) { return __builtin_bit_cast(float, x); }
__device__ __forceinline__ unsigned pack(float a, float b) { return (f2u(a) >> 16) | (f2u(b) & 0xffff0000u); }
__device__ __forceinline__ unsigned canon(unsigned u) { return u == SENT ? 0x7fc07fc0u : u; }
__device__ __forceinline__ float dot2(unsigned k, unsigned q, float acc) {
  return __builtin_amdgcn_fdot2_f32_bf16(__builtin_bit_cast(bf16x2, k), __builtin_bit_cast(bf16x2, q), acc, false);
}
template <int CTRL> __device__ __forceinline__ float dpp(float x) {
  return u2f(__builtin_amdgcn_mov_dpp(f2u(x), CTRL, 0xf, 0xf, true));
}
constexpr int XOR1 = 0xB1, XOR2 = 0x4E, HMIR = 0x141, RMIR = 0x140, ROR8 = 0x128, SHL4 = 0x104;
__device__ __forceinline__ float silu(float x) { return x / (1.f + __expf(-x)); }
__device__ __forceinline__ float red32(float a, float b) {
  auto r = __builtin_amdgcn_permlane32_swap(f2u(a), f2u(b), false, false);
  return u2f(r[0]) + u2f(r[1]);
}
__device__ __forceinline__ float red16(float a, float b) {
  auto r = __builtin_amdgcn_permlane16_swap(f2u(a), f2u(b), false, false);
  return u2f(r[0]) + u2f(r[1]);
}
__device__ __forceinline__ float red8(float a, float b, bool bit3) {
  const float keep = bit3 ? b : a, send = bit3 ? a : b;
  return keep + dpp<ROR8>(send);
}
__device__ __forceinline__ float max32(float a) {
  auto r = __builtin_amdgcn_permlane32_swap(f2u(a), f2u(a), false, false);
  return fmaxf(u2f(r[0]), u2f(r[1]));
}
__device__ __forceinline__ float max16(float a) {
  auto r = __builtin_amdgcn_permlane16_swap(f2u(a), f2u(a), false, false);
  return fmaxf(u2f(r[0]), u2f(r[1]));
}
__device__ __forceinline__ float wave_sum(float v) {
  v += dpp<XOR1>(v); v += dpp<XOR2>(v); v += dpp<HMIR>(v); v += dpp<RMIR>(v);
  v = red16(v, v); v = red32(v, v);
  return v;
}
__device__ __forceinline__ float red_oct(const float (&a)[8], bool bit3) {
  const float b0 = red32(a[0], a[4]), b1 = red32(a[1], a[5]), b2 = red32(a[2], a[6]), b3 = red32(a[3], a[7]);
  const float c0 = red16(b0, b2), c1 = red16(b1, b3);
  return red8(c0, c1, bit3);
}
__device__ __forceinline__ void mac2(float (&acc)[8], const u32x4& w0, const u32x4& w1, unsigned ap) {
#pragma unroll
  for (int i = 0; i < 4; ++i) {
    acc[2 * i] = dot2(__builtin_amdgcn_perm(w1[i], w0[i], 0x05040100u), ap, acc[2 * i]);
    acc[2 * i + 1] = dot2(__builtin_amdgcn_perm(w1[i], w0[i], 0x07060302u), ap, acc[2 * i + 1]);
  }
}
__device__ __forceinline__ bool valid4(const u32x4& v) { return v[0] != SENT && v[1] != SENT && v[2] != SENT && v[3] != SENT; }
__device__ __forceinline__ unsigned umax3(unsigned a, unsigned b, unsigned c) { return max(max(a, b), c); }
__device__ __forceinline__ unsigned umax4(const u32x4& v) { return umax3(v[0], v[1], max(v[2], v[3])); }
__device__ __forceinline__ u64 ld_gran(const char* ws, unsigned idx) {
  return __hip_atomic_load((const u64*)(ws + W_MODG) + idx, __ATOMIC_RELAXED, __HIP_MEMORY_SCOPE_AGENT);
}
#define MEMFENCE() asm volatile("" ::: "memory")
#define WAIT_VM(n) __builtin_amdgcn_s_waitcnt(0x0F70 | ((n) & 15) | (((n) >> 4) << 14))
__device__ __forceinline__ void wg_barrier() {
  asm volatile("s_waitcnt lgkmcnt(0)\n\ts_barrier" ::: "memory");
}
__device__ __forceinline__ void lds_arrive(unsigned* cnt, int lane) {
  asm volatile("s_waitcnt lgkmcnt(0)" ::: "memory");
  if (lane == 0) __hip_atomic_fetch_add(cnt, 1u, __ATOMIC_RELAXED, __HIP_MEMORY_SCOPE_WORKGROUP);
  MEMFENCE();
}
__device__ __forceinline__ void lds_wait(unsigned* cnt, unsigned target) {
  MEMFENCE();
  _Pragma("nounroll") for (unsigned i = 0; i < (1u << 24); ++i) {
    if (__hip_atomic_load(cnt, __ATOMIC_RELAXED, __HIP_MEMORY_SCOPE_WORKGROUP) >= target) break;
    __builtin_amdgcn_s_sleep(1);
  }
  MEMFENCE();
}

struct Tile { u32x4 k[3], v[3]; };
__device__ __forceinline__ void ld_tile(Tile& t, rsrc_t rk, rsrc_t rv, unsigned voff, unsigned soff) {
#pragma unroll
  for (int j = 0; j < 3; ++j) t.k[j] = __builtin_amdgcn_raw_buffer_load_b128(rk, voff + j * 128, soff, 2);
#pragma unroll
  for (int j = 0; j < 3; ++j) t.v[j] = __builtin_amdgcn_raw_buffer_load_b128(rv, voff + j * 128, soff, 2);
  MEMFENCE();
  __builtin_amdgcn_sched_barrier(0);
}
__device__ __forceinline__ void ld_tile_lds(unsigned ldst, rsrc_t rk, rsrc_t rv, unsigned v0, unsigned v1, unsigned v2, unsigned soff) {
  unsigned keep;
  asm volatile("s_mov_b32 %0, m0\n\t"
               "s_mov_b32 m0, %1\n\ts_nop 0\n\tbuffer_load_dwordx4 %2, %5, %7 offen nt lds\n\t"
               "s_addk_i32 m0, 0x400\n\ts_nop 0\n\tbuffer_load_dwordx4 %3, %5, %7 offen nt lds\n\t"
               "s_addk_i32 m0, 0x400\n\ts_nop 0\n\tbuffer_load_dwordx4 %4, %5, %7 offen nt lds\n\t"
               "s_addk_i32 m0, 0x400\n\ts_nop 0\n\tbuffer_load_dwordx4 %2, %6, %7 offen nt lds\n\t"
               "s_addk_i32 m0, 0x400\n\ts_nop 0\n\tbuffer_load_dwordx4 %3, %6, %7 offen nt lds\n\t"
               "s_addk_i32 m0, 0x400\n\ts_nop 0\n\tbuffer_load_dwordx4 %4, %6, %7 offen nt lds\n\t"
               "s_mov_b32 m0, %0"
               : "=&s"(keep) : "s"(ldst), "v"(v0), "v"(v1), "v"(v2), "s"(rk), "s"(rv), "s"(soff) : "memory", "scc");
}
__device__ __forceinline__ void use_tile(const Tile& t, const unsigned (&qp)[12], float& m, float& l, float (&acc)[24]) {
  float d = 0.f;
#pragma unroll
  for (int j = 0; j < 3; ++j)
#pragma unroll
    for (int i = 0; i < 4; ++i) d = dot2(t.k[j][i], qp[j * 4 + i], d);
  d += dpp<XOR1>(d); d += dpp<XOR2>(d); d += dpp<HMIR>(d);
  const float sc = rbf(rbf(d) * 0.07216878364870322f);
  const float mn = fmaxf(m, sc);
  const float alpha = __expf(m - mn), p = __expf(sc - mn);
  m = mn; l = l * alpha + p;
#pragma unroll
  for (int j = 0; j < 3; ++j)
#pragma unroll
    for (int i = 0; i < 4; ++i) {
      acc[j * 8 + 2 * i] = fmaf(p, lo(t.v[j][i]), acc[j * 8 + 2 * i] * alpha);
      acc[j * 8 + 2 * i + 1] = fmaf(p, hi(t.v[j][i]), acc[j * 8 + 2 * i + 1] * alpha);
    }
}

#define STAMP(i) do { } while (0)

__global__ void __launch_bounds__(512, 2)
fused_block(const u16* __restrict__ x, const u16* __restrict__ Kc, const u16* __restrict__ Vc, const u16* __restrict__ wq,
            const u16* __restrict__ wo, const u16* __restrict__ wg, const u16* __restrict__ wu, const u16* __restrict__ wd,
            const u16* __restrict__ cvec, const u16* __restrict__ wmod, u16* __restrict__ out, char* __restrict__ ws) {
  __shared__ __attribute__((aligned(16))) unsigned s_flag[8];
  __shared__ __attribute__((aligned(16))) unsigned s_hp[384];
  __shared__ __attribute__((aligned(16))) float s_red[8][32];
  __shared__ __attribute__((aligned(16))) unsigned s_q[192];
  __shared__ __attribute__((aligned(16))) unsigned s_big[(NL > 2 ? NL : 2) * 4 * 1536];
  __shared__ __attribute__((aligned(16))) float s_ml[16][8];
  __shared__ __attribute__((aligned(16))) float s_x1[D];

  const int tid = threadIdx.x, lane = tid & 63, wave = __builtin_amdgcn_readfirstlane(tid >> 6);
  const int b = blockIdx.x, s = b & 7, h = b >> 3;
  const int h0 = h & 15, h1 = h0 + 16, w16 = (h >> 4) * 8 + s;
  float (*s_mrg)[4 * DH] = (float (*)[4 * DH])s_big;
  const bool bit3 = (lane & 8) != 0;
  const rsrc_t rw = __builtin_amdgcn_make_buffer_rsrc(ws, 0, W_USED, 0x00020000);
  const rsrc_t rk0 = __builtin_amdgcn_make_buffer_rsrc((char*)Kc + (size_t)h0 * SK * DH * 2, 0, SK * DH * 2, 0x00020000);
  const rsrc_t rv0 = __builtin_amdgcn_make_buffer_rsrc((char*)Vc + (size_t)h0 * SK * DH * 2, 0, SK * DH * 2, 0x00020000);
  const rsrc_t rk1 = __builtin_amdgcn_make_buffer_rsrc((char*)Kc + (size_t)h1 * SK * DH * 2, 0, SK * DH * 2, 0x00020000);
  const rsrc_t rv1 = __builtin_amdgcn_make_buffer_rsrc((char*)Vc + (size_t)h1 * SK * DH * 2, 0, SK * DH * 2, 0x00020000);
  const unsigned voff = (lane >> 3) * 384 + (lane & 7) * 16;
  const bool roleA = s < 5, qprod = s < 6;
  const int ag = (s * 32 + h) / 20, ak = (s * 32 + h) % 20;
  const int bc = (s - 5) * 32 + h;
  const int cc = bc;
  const bool roleC = s >= 5 && cc < 40;
  const int qc = s * 32 + h;
  const int cg = tid & 3, rs = tid >> 2;
  const int pb = w16 * 4 + (wave - 4), gb = NPS * 64 + w16 * 8 + wave;
  auto so0 = [&](int idx) -> unsigned { return (unsigned)(((idx + h0 * STAG) & (NT - 1)) * TILEB); };
  auto so1 = [&](int idx) -> unsigned { return (unsigned)(((idx + h1 * STAG) & (NT - 1)) * TILEB); };

  STAMP(0);
  Tile A, B, P[NP];
  const unsigned ep = __hip_atomic_load((const unsigned*)(ws + W_EPOCH), __ATOMIC_RELAXED, __HIP_MEMORY_SCOPE_AGENT);
  unsigned tag;
  if (wave >= 4) {
    if (wave == 4) {
      const u32x4 s4 = {SENT, SENT, SENT, SENT};
      __builtin_amdgcn_raw_buffer_store_b128(s4, rw, lane < 48 ? W_PART + (h0 * 16 + w16) * (DH * 4) + lane * 16 : OOB, 0, 16);
      __builtin_amdgcn_raw_buffer_store_b128(s4, rw, lane < 48 ? W_PART + (h1 * 16 + w16) * (DH * 4) + lane * 16 : OOB, 0, 16);
      unsigned off = OOB, val = SENT;
      if (lane < 16) { if (qprod) off = W_QS + (qc * 16 + lane) * 4; }
      else if (lane < 48) { if (roleA) off = W_YP + (ag * D + ak * 32 + (lane - 16)) * 4; }
      else if (lane < 56) { if (!roleA) off = W_ACT + (bc * 8 + (lane - 48)) * 4; }
      else if (lane < 60) off = W_ML + ((lane & 2) ? h1 : h0) * 128 + w16 * 8 + (lane & 1) * 4;
      else if (lane == 60) { if (w16 == 0) { off = W_CNT + h0 * 256; val = 0; } }
      __builtin_amdgcn_raw_buffer_store_b32(val, rw, off, 0, 16);
      if (lane < 8) s_flag[lane] = 0;
    }
    wg_barrier();
    tag = ep * 1664525u + 1013904223u;
    if (wave == 4) {
      asm volatile("s_waitcnt vmcnt(0)" ::: "memory");
      if (lane == 0) __hip_atomic_store((u64*)(ws + W_RDY) + b, (u64)tag << 32, __ATOMIC_RELAXED, __HIP_MEMORY_SCOPE_AGENT);
    }
#pragma unroll
    for (int i = 0; i < NP; ++i) {
      if (i & 1) ld_tile(P[i], rk1, rv1, voff, so1(pb + 64 * (i >> 1))); else ld_tile(P[i], rk0, rv0, voff, so0(pb + 64 * (i >> 1)));
      if (i) WAIT_VM(6);
    }
    {
      const unsigned lbase = (unsigned)(uintptr_t)(__attribute__((address_space(3))) unsigned*)s_big + (wave - 4) * (NL * 6144);
#pragma unroll
      for (int i = 0; i < NL; ++i) {
        const int st = pb + 64 * ((NP + i) >> 1);
        if (i & 1) ld_tile_lds(lbase + i * 6144, rk1, rv1, voff, voff + 128, voff + 256, so1(st));
        else ld_tile_lds(lbase + i * 6144, rk0, rv0, voff, voff + 128, voff + 256, so0(st));
        WAIT_VM(6);
      }
    }
    ld_tile(A, rk0, rv0, voff, so0(gb));
    WAIT_VM(6);
    ld_tile(B, rk1, rv1, voff, so1(gb));
    STAMP(1);
  } else {
    const rsrc_t ru = qprod ? __builtin_amdgcn_make_buffer_rsrc((void*)wq, 0, D * HD * 2, 0x00020000)
                            : __builtin_amdgcn_make_buffer_rsrc((void*)wmod, 0, D * NMOD * 2, 0x00020000);
    const unsigned rstride = qprod ? HD * 2 : NMOD * 2;
    const int mu = (s - 6) * 32 + h;
    u32x4 wt[10];
    unsigned av[5];
    {
      const rsrc_t ra = __builtin_amdgcn_make_buffer_rsrc((void*)(qprod ? x : cvec), 0, D * 2, 0x00020000);
      const unsigned a0 = qprod ? lane * 4 : rs * 20, as = qprod ? 256 : 4;
#pragma unroll
      for (int i = 0; i < 5; ++i) av[i] = __builtin_amdgcn_raw_buffer_load_b32(ra, a0 + as * i, 0, 0);
      const unsigned so = qprod ? qc * 64 : mu * 64;
#pragma unroll
      for (int i = 0; i < 10; ++i) wt[i] = __builtin_amdgcn_raw_buffer_load_b128(ru, rs * 10 * rstride + cg * 16, so + i * rstride, 0);
    }
    MEMFENCE();
    wg_barrier();
    tag = ep * 1664525u + 1013904223u;
    unsigned arrivals = 0;
    auto gemv = [&](const unsigned (&ap)[5]) -> float {
      float acc[8];
#pragma unroll
      for (int i = 0; i < 8; ++i) acc[i] = 0.f;
#pragma unroll
      for (int u = 0; u < 5; ++u) mac2(acc, wt[2 * u], wt[2 * u + 1], ap[u]);
      float v = red_oct(acc, bit3);
      v += dpp<SHL4>(v);
      float* red = &s_red[(arrivals & 4) + wave][0];
      if ((lane & 4) == 0) red[cg * 8 + (lane >> 3)] = v;
      lds_arrive(&s_flag[1], lane);
      float r = 0.f;
      if (wave == 0) {
        lds_wait(&s_flag[1], arrivals + 4);
        const int cI = lane & 31, o = arrivals & 4;
        r = rbf((s_red[o][cI] + s_red[o + 1][cI]) + (s_red[o + 2][cI] + s_red[o + 3][cI]));
      }
      arrivals += 4;
      return r;
    };
    float n0 = 0.f, n1 = 0.f, n2 = 0.f, n3 = 0.f;
    if (!qprod) {
      unsigned ap[5];
#pragma unroll
      for (int u = 0; u < 5; ++u) ap[u] = pack(rbf(silu(lo(av[u]))), rbf(silu(hi(av[u]))));
#pragma nounroll
      for (int rnd = 0; rnd < 2; ++rnd) {
        const int unit = rnd * 64 + mu;
        const float r = gemv(ap);
        if (rnd == 0) {
          const unsigned so = mu + 64 < 120 ? (mu + 64) * 64 : OOB;
#pragma unroll
          for (int i = 0; i < 10; ++i) wt[i] = __builtin_amdgcn_raw_buffer_load_b128(ru, rs * 10 * rstride + cg * 16, so + i * rstride, 0);
        }
        const unsigned pr = pack(r, dpp<XOR1>(r));
        if (wave == 0 && lane < 32 && (lane & 1) == 0 && unit < 120)
          __hip_atomic_store((u64*)(ws + W_MODG) + unit * 16 + (lane >> 1), ((u64)tag << 32) | pr, __ATOMIC_RELAXED, __HIP_MEMORY_SCOPE_AGENT);
        if (rnd == 0) STAMP(1);
      }
    } else {
      float sum = 0.f;
#pragma unroll
      for (int i = 0; i < 5; ++i) sum += lo(av[i]) + hi(av[i]);
      const float mean = wave_sum(sum) * (1.f / D);
      float sq = 0.f;
#pragma unroll
      for (int i = 0; i < 5; ++i) { const float a = lo(av[i]) - mean, c2 = hi(av[i]) - mean; sq += a * a + c2 * c2; }
      const float var = wave_sum(sq) * (1.f / D);
      const float mb = rbf(mean), den = rbf(sqrtf(rbf(rbf(var) + 1e-5f)));
      const unsigned xa = wave == 0 ? av[0] : wave == 1 ? av[1] : wave == 2 ? av[2] : av[3];
      n0 = rbf(rbf(lo(xa) - mb) / den); n1 = rbf(rbf(hi(xa) - mb) / den);
      n2 = rbf(rbf(lo(av[4]) - mb) / den); n3 = rbf(rbf(hi(av[4]) - mb) / den);
      STAMP(1);
    }
    {
      const unsigned i0 = tid, i1 = 320 + tid, i2 = 1920 + tid;
      const unsigned i3 = wave == 0 ? 256 + tid : i0, i4 = wave == 0 ? 576 + tid : i1;
      u64 g0, g1, g2, g3, g4;
      _Pragma("nounroll") for (unsigned it = 0; it < SPIN_CAP; ++it) {
        g0 = ld_gran(ws, i0); g1 = ld_gran(ws, i1); g2 = ld_gran(ws, i2); g3 = ld_gran(ws, i3); g4 = ld_gran(ws, i4);
        const bool ok = (unsigned)(g0 >> 32) == tag && (unsigned)(g1 >> 32) == tag && (unsigned)(g2 >> 32) == tag &&
                        (unsigned)(g3 >> 32) == tag && (unsigned)(g4 >> 32) == tag;
        if (__all(ok)) break;
        __builtin_amdgcn_s_sleep(1);
      }
      const unsigned sa = (unsigned)g0, ca = (unsigned)g1;
      s_hp[tid] = pack(rbf(rbf(n0 * rbf(1.f + lo(ca))) + lo(sa)), rbf(rbf(n1 * rbf(1.f + hi(ca))) + hi(sa)));
      if (wave == 0) {
        const unsigned sa2 = (unsigned)g3, ca2 = (unsigned)g4;
        s_hp[256 + tid] = pack(rbf(rbf(n2 * rbf(1.f + lo(ca2))) + lo(sa2)), rbf(rbf(n3 * rbf(1.f + hi(ca2))) + hi(sa2)));
      }
    }
    STAMP(2);
    lds_arrive(&s_flag[0], lane);
    if (qprod || wave == 0) lds_wait(&s_flag[0], 4);
    if (qprod) {
      unsigned ap[5];
#pragma unroll
      for (int u = 0; u < 5; ++u) ap[u] = s_hp[rs * 5 + u];
      const float r = gemv(ap);
      const unsigned pr = canon(pack(r, dpp<XOR1>(r)));
      if (wave == 0)
        __builtin_amdgcn_raw_buffer_store_b32(pr, rw, (lane < 32 && (lane & 1) == 0) ? W_QS + (qc * 16 + (lane >> 1)) * 4 : OOB, 0, 16);
    }
    STAMP(3);
    if (wave == 0) {
      u32x4 qv;
      _Pragma("nounroll") for (unsigned it = 0; it < SPIN_CAP; ++it) {
        MEMFENCE();
        qv = __builtin_amdgcn_raw_buffer_load_b128(rw, lane < 24 ? W_QS + h0 * 384 + lane * 16 : lane < 48 ? W_QS + h1 * 384 + (lane - 24) * 16 : OOB, 0, 16);
        if (__all(umax4(qv) != SENT)) break;
        __builtin_amdgcn_s_sleep(1);
      }
      if (lane < 48) *(u32x4*)&s_q[lane * 4] = qv;
      lds_arrive(&s_flag[2], lane);
    }
    ld_tile(A, rk0, rv0, voff, so0(gb));
    ld_tile(B, rk1, rv1, voff, so1(gb));
  }
  lds_wait(&s_flag[2], 1);
  STAMP(4);
  unsigned q0[12], q1[12];
#pragma unroll
  for (int j = 0; j < 3; ++j) {
    const u32x4 t0 = *(const u32x4*)&s_q[j * 32 + (lane & 7) * 4], t1 = *(const u32x4*)&s_q[96 + j * 32 + (lane & 7) * 4];
#pragma unroll
    for (int i = 0; i < 4; ++i) { q0[j * 4 + i] = t0[i]; q1[j * 4 + i] = t1[i]; }
  }
  float m0 = -INFINITY, l0 = 0.f, acc0[24], m1 = -INFINITY, l1 = 0.f, acc1[24];
#pragma unroll
  for (int i = 0; i < 24; ++i) { acc0[i] = 0.f; acc1[i] = 0.f; }
  const unsigned coff = lane == 0 ? W_CNT + h0 * 256 : OOB;
  unsigned pend = (unsigned)__builtin_amdgcn_raw_ptr_buffer_atomic_add_i32(1, rw, coff, 0, 0);
  if (wave >= 4) { use_tile(P[0], q0, m0, l0, acc0); use_tile(P[1], q1, m1, l1, acc1); }
  MEMFENCE();
  __builtin_amdgcn_sched_barrier(0);
  use_tile(A, q0, m0, l0, acc0);
  ld_tile(A, rk0, rv0, voff, so0(gb + 128));
  use_tile(B, q1, m1, l1, acc1);
  ld_tile(B, rk1, rv1, voff, so1(gb + 128));
  if (wave >= 4) {
#pragma unroll
    for (int i = 2; i < NP; ++i) { if (i & 1) use_tile(P[i], q1, m1, l1, acc1); else use_tile(P[i], q0, m0, l0, acc0); }
    const unsigned* lb = s_big + (wave - 4) * (NL * 1536) + lane * 4;
#pragma unroll
    for (int i = 0; i < NL; ++i) {
      Tile T;
#pragma unroll
      for (int j = 0; j < 3; ++j) { T.k[j] = *(const u32x4*)(lb + i * 1536 + j * 256); T.v[j] = *(const u32x4*)(lb + i * 1536 + 768 + j * 256); }
      if (i & 1) use_tile(T, q1, m1, l1, acc1); else use_tile(T, q0, m0, l0, acc0);
    }
  }
  if (wave >= 4) lds_arrive(&s_flag[4], lane);
  u32x4 wa[6], wc[6];
  const int bp0 = rs < 64 ? rs * 3 : 192 + (rs - 64) * 2;
  {
    if (roleA) {
#pragma unroll
      for (int i = 0; i < 6; ++i) wa[i] = *(const u32x4*)(wo + (size_t)(ag * 768 + rs * 6 + i) * D + ak * 32 + cg * 8);
    } else {
      const u16* wgu = ((cg & 2) ? wu : wg) + (size_t)bp0 * 2 * F + bc * 16 + (cg & 1) * 8;
#pragma unroll
      for (int i = 0; i < 4; ++i) wa[i] = *(const u32x4*)(wgu + i * F);
      const int ex = wave < 4 ? 4 : 0;
      wa[4] = *(const u32x4*)(wgu + ex * F);
      wa[5] = *(const u32x4*)(wgu + (ex + 1) * F);
    }
    if (roleC) {
#pragma unroll
      for (int i = 0; i < 6; ++i) wc[i] = *(const u32x4*)(wd + (size_t)((tid >> 1) * 6 + i) * D + cc * 16 + (tid & 1) * 8);
    }
    MEMFENCE();
    __builtin_amdgcn_sched_barrier(0);
  }
  STAMP(5);
  while (true) {
    const int tn = NSTAT + __builtin_amdgcn_readfirstlane(pend);
    const unsigned o0 = so0(tn), o1 = so1(tn);
    use_tile(A, q0, m0, l0, acc0);
    MEMFENCE();
    __builtin_amdgcn_sched_barrier(0);
    pend = (unsigned)__builtin_amdgcn_raw_ptr_buffer_atomic_add_i32(1, rw, coff, 0, 0);
    ld_tile(A, rk0, rv0, voff, tn >= NT ? OOB : o0);
    use_tile(B, q1, m1, l1, acc1);
    ld_tile(B, rk1, rv1, voff, tn >= NT ? OOB : o1);
    if (tn >= NT) break;
  }
  asm volatile("" :: "v"(A.k[0]), "v"(A.k[1]), "v"(A.k[2]), "v"(A.v[0]), "v"(A.v[1]), "v"(A.v[2]),
                     "v"(B.k[0]), "v"(B.k[1]), "v"(B.k[2]), "v"(B.v[0]), "v"(B.v[1]), "v"(B.v[2]));
  STAMP(6);
  lds_wait(&s_flag[4], 4);
  auto wave_merge = [&](float m, float l, const float (&acc)[24], int slot) {
    float M = fmaxf(m, dpp<ROR8>(m)); M = max16(M); M = max32(M);
    const float w = __expf(m - M);
    l *= w;
    l += dpp<ROR8>(l); l = red16(l, l); l = red32(l, l);
    float r12[12], r6[6];
#pragma unroll
    for (int i = 0; i < 12; ++i) r12[i] = red32(acc[i] * w, acc[i + 12] * w);
#pragma unroll
    for (int i = 0; i < 6; ++i) r6[i] = red16(r12[i], r12[i + 6]);
    const int ebase = 12 * ((lane >> 5) & 1) + 6 * ((lane >> 4) & 1) + 3 * ((lane >> 3) & 1);
#pragma unroll
    for (int i = 0; i < 3; ++i) {
      const float v = red8(r6[i], r6[i + 3], bit3);
      const int e = ebase + i;
      s_mrg[wave][slot * 256 + (e >> 3) * 64 + (lane & 7) * 8 + (e & 7)] = v;
    }
    if (lane == 0) { s_ml[slot * 2][wave] = M; s_ml[slot * 2 + 1][wave] = l; }
  };
  wave_merge(m0, l0, acc0, 0);
  wave_merge(m1, l1, acc1, 1);
  wg_barrier();
  {
    const int sl = wave >> 2, t = tid & 255, hd = sl ? h1 : h0;
    float M = s_ml[sl * 2][0];
#pragma unroll
    for (int w = 1; w < 8; ++w) M = fmaxf(M, s_ml[sl * 2][w]);
    float L = 0.f, o = 0.f;
#pragma unroll
    for (int w = 0; w < 8; ++w) { const float e = __expf(s_ml[sl * 2][w] - M); L = fmaf(e, s_ml[sl * 2 + 1][w], L); o = fmaf(e, s_mrg[w][tid], o); }
    unsigned off = OOB; float val = o;
    if (t < DH) off = W_PART + ((hd * 16 + w16) * DH + t) * 4;
    else if (t == DH) { off = W_ML + hd * 128 + w16 * 8; val = M; }
    else if (t == DH + 1) { off = W_ML + hd * 128 + w16 * 8 + 4; val = L; }
    __builtin_amdgcn_raw_buffer_store_b32(canon(f2u(val)), rw, off, 0, 16);
  }
  STAMP(7);
  if (roleA) {
    wg_barrier();
    {
      unsigned po[3];
#pragma unroll
      for (int i = 0; i < 3; ++i) { const int qd = lane + 64 * i, hh = qd / 48; po[i] = W_PART + (((ag * 4 + hh) * 16 + wave) * DH + (qd - hh * 48) * 4) * 4; }
      const unsigned mo = lane < 8 ? W_ML + (ag * 4 + (lane & 3)) * 128 + (wave + (lane >> 2) * 8) * 8 : OOB;
      u32x4 a[6]; u32x2 ml;
      _Pragma("nounroll") for (unsigned it = 0; it < SPIN_CAP; ++it) {
        MEMFENCE();
        ml = __builtin_amdgcn_raw_buffer_load_b64(rw, mo, 0, 16);
#pragma unroll
        for (int i = 0; i < 6; ++i) a[i] = __builtin_amdgcn_raw_buffer_load_b128(rw, po[i % 3], (i / 3) * (8 * DH * 4), 16);
        const unsigned mx = umax3(umax3(umax4(a[0]), umax4(a[1]), umax4(a[2])), umax3(umax4(a[3]), umax4(a[4]), umax4(a[5])), max(ml[0], ml[1]));
        if (__all(mx != SENT)) break;
        __builtin_amdgcn_s_sleep(POLL_A);
      }
#pragma unroll
      for (int i = 0; i < 6; ++i) *(u32x4*)&s_mrg[wave + (i / 3) * 8][(i % 3) * 256 + lane * 4] = a[i];
      if (lane < 8) *(u32x2*)&s_ml[wave + (lane >> 2) * 8][(lane & 3) * 2] = ml;
    }
    STAMP(8);
    wg_barrier();
    if (tid < 384) {
      const int hh = tid / 96;
      float mv[16], M = -INFINITY;
#pragma unroll
      for (int i = 0; i < 16; ++i) { mv[i] = s_ml[i][hh * 2]; M = fmaxf(M, mv[i]); }
      float L = 0.f, o0 = 0.f, o1 = 0.f;
#pragma unroll
      for (int i = 0; i < 16; ++i) {
        const float e = __expf(mv[i] - M);
        const float2 a = *(const float2*)&s_mrg[i][tid * 2];
        L = fmaf(e, s_ml[i][hh * 2 + 1], L); o0 = fmaf(e, a.x, o0); o1 = fmaf(e, a.y, o1);
      }
      const float inv = 1.f / L;
      s_hp[tid] = pack(rbf(o0 * inv), rbf(o1 * inv));
    }
    wg_barrier();
    float ac[8];
#pragma unroll
    for (int i = 0; i < 8; ++i) ac[i] = 0.f;
#pragma unroll
    for (int u = 0; u < 3; ++u) mac2(ac, wa[2 * u], wa[2 * u + 1], s_hp[rs * 3 + u]);
    float v = red_oct(ac, bit3);
    v += dpp<SHL4>(v);
    if ((lane & 4) == 0) s_red[wave][cg * 8 + (lane >> 3)] = v;
    wg_barrier();
    if (tid < 32) {
      float y = 0.f;
#pragma unroll
      for (int w = 0; w < 8; ++w) y += s_red[w][tid];
      __builtin_amdgcn_raw_buffer_store_b32(canon(f2u(y)), rw, W_YP + (ag * D + ak * 32 + tid) * 4, 0, 16);
    }
    STAMP(9);
  } else {
    const int t2 = tid < 320 ? tid : 0;
    unsigned gga, gsb, gcb, ggb;
    const unsigned xpair = ((const unsigned*)x)[t2];
    {
      u64 g0, g1, g2, g3;
      const unsigned i3 = 1600 + (roleC ? cc * 8 : 0) + ((lane & 15) >> 1);
      _Pragma("nounroll") for (unsigned it = 0; it < SPIN_CAP; ++it) {
        g0 = ld_gran(ws, 640 + t2); g1 = ld_gran(ws, 960 + t2); g2 = ld_gran(ws, 1280 + t2); g3 = ld_gran(ws, i3);
        if (__all((unsigned)(g0 >> 32) == tag && (unsigned)(g1 >> 32) == tag && (unsigned)(g2 >> 32) == tag && (unsigned)(g3 >> 32) == tag)) break;
        __builtin_amdgcn_s_sleep(2);
      }
      gga = (unsigned)g0; gsb = (unsigned)g1; gcb = (unsigned)g2; ggb = (unsigned)g3;
    }
    const float c1a = rbf(1.f + lo(gcb)), c1b = rbf(1.f + hi(gcb));
    float xa = 0.f, xb = 0.f;
    if (wave < 5) {
      _Pragma("nounroll") for (unsigned it = 0; it < SPIN_CAP; ++it) {
        MEMFENCE();
        const u32x4 t0 = __builtin_amdgcn_raw_buffer_load_b128(rw, W_ML + lane * 32, 0, 16);
        const u32x4 t1 = __builtin_amdgcn_raw_buffer_load_b128(rw, W_ML + lane * 32 + 16, 0, 16);
        if (__all(max(umax4(t0), umax4(t1)) != SENT)) break;
        __builtin_amdgcn_s_sleep(8);
      }
      u32x2 yp[8];
      _Pragma("nounroll") for (unsigned it = 0; it < SPIN_CAP; ++it) {
        MEMFENCE();
#pragma unroll
        for (int i = 0; i < 8; ++i) yp[i] = __builtin_amdgcn_raw_buffer_load_b64(rw, W_YP + t2 * 8, i * (D * 4), 16);
        unsigned mx = 0;
#pragma unroll
        for (int i = 0; i < 8; ++i) mx = umax3(mx, yp[i][0], yp[i][1]);
        if (__all(mx != SENT)) break;
        __builtin_amdgcn_s_sleep(POLL_B);
      }
      float y0 = 0.f, y1 = 0.f;
#pragma unroll
      for (int i = 0; i < 8; ++i) { y0 += u2f(yp[i][0]); y1 += u2f(yp[i][1]); }
      xa = rbf(lo(xpair) + rbf(lo(gga) * rbf(y0)));
      xb = rbf(hi(xpair) + rbf(hi(gga) * rbf(y1)));
      const float k = u2f(__builtin_amdgcn_readfirstlane(f2u(xa)));
      const float da = xa - k, db = xb - k;
      const float S = wave_sum(da + db), Q = wave_sum(fmaf(da, da, db * db));
      *(float2*)&s_x1[tid * 2] = make_float2(xa, xb);
      if (lane == 0) { s_ml[4][wave] = k + S * (1.f / 128); s_ml[5][wave] = Q - S * S * (1.f / 128); }
    }
    STAMP(8);
    wg_barrier();
    if (wave < 5) {
      float mw[5], mean = 0.f, M2 = 0.f;
#pragma unroll
      for (int w = 0; w < 5; ++w) { mw[w] = s_ml[4][w]; mean += mw[w]; M2 += s_ml[5][w]; }
      mean *= 0.2f;
#pragma unroll
      for (int w = 0; w < 5; ++w) { const float dd = mw[w] - mean; M2 = fmaf(128.f * dd, dd, M2); }
      const float mb = rbf(mean), den = rbf(sqrtf(rbf(rbf(M2 * (1.f / D)) + 1e-5f)));
      const float na = rbf(rbf(xa - mb) / den), nb = rbf(rbf(xb - mb) / den);
      s_hp[tid] = pack(rbf(rbf(na * c1a) + lo(gsb)), rbf(rbf(nb * c1b) + hi(gsb)));
    }
    wg_barrier();
    float ac[8];
#pragma unroll
    for (int i = 0; i < 8; ++i) ac[i] = 0.f;
    mac2(ac, wa[0], wa[1], s_hp[bp0]);
    mac2(ac, wa[2], wa[3], s_hp[bp0 + 1]);
    if (wave < 4) mac2(ac, wa[4], wa[5], s_hp[bp0 + 2]);
    float v = red_oct(ac, bit3);
    v += dpp<SHL4>(v);
    if ((lane & 4) == 0) s_red[wave][cg * 8 + (lane >> 3)] = v;
    wg_barrier();
    if (wave == 0) {
      const int j = lane & 15;
      float g = 0.f, u = 0.f;
#pragma unroll
      for (int w = 0; w < 8; ++w) { g += s_red[w][j]; u += s_red[w][16 + j]; }
      const float av = rbf(rbf(silu(rbf(g))) * rbf(u));
      const unsigned pr = canon(pack(av, dpp<XOR1>(av)));
      __builtin_amdgcn_raw_buffer_store_b32(pr, rw, (lane < 16 && (lane & 1) == 0) ? W_ACT + (bc * 8 + (lane >> 1)) * 4 : OOB, 0, 16);
    }
    STAMP(9);
    if (roleC) {
      u32x3 ap;
      _Pragma("nounroll") for (unsigned it = 0; it < SPIN_CAP; ++it) {
        MEMFENCE();
        ap = __builtin_amdgcn_raw_buffer_load_b96(rw, W_ACT + (tid >> 1) * 12, 0, 16);
        if (__all(umax3(ap[0], ap[1], ap[2]) != SENT)) break;
        __builtin_amdgcn_s_sleep(1);
      }
      STAMP(10);
      float dc[8];
#pragma unroll
      for (int i = 0; i < 8; ++i) dc[i] = 0.f;
#pragma unroll
      for (int u = 0; u < 3; ++u) mac2(dc, wc[2 * u], wc[2 * u + 1], ap[u]);
      float dv = red_oct(dc, bit3);
      dv += dpp<SHL4>(dv); dv += dpp<XOR2>(dv);
      if ((lane & 6) == 0) s_mrg[wave][(lane & 1) * 8 + (lane >> 3)] = dv;
      wg_barrier();
      if (tid < 16) {
        float dn = 0.f;
#pragma unroll
        for (int w = 0; w < 8; ++w) dn += s_mrg[w][tid];
        const int n = cc * 16 + tid;
        const float gbv = (tid & 1) ? hi(ggb) : lo(ggb);
        const float r = s_x1[n] + rbf(gbv * rbf(dn));
        out[n] = (u16)(f2u(rbf(r)) >> 16);
        if (cc == 0 && tid == 0) __hip_atomic_store((unsigned*)(ws + W_EPOCH), tag, __ATOMIC_RELAXED, __HIP_MEMORY_SCOPE_AGENT);
      }
      STAMP(11);
    }
  }
}

extern "C" void kernel_launch(void* const* d_in, const int* in_sizes, int n_in, void* d_out, int out_size, void* d_ws, size_t ws_size,
                              hipStream_t stream) {
  fused_block<<<256, 512, 0, stream>>>((const u16*)d_in[0], (const u16*)d_in[1], (const u16*)d_in[2], (const u16*)d_in[3],
                                       (const u16*)d_in[4], (const u16*)d_in[6], (const u16*)d_in[7], (const u16*)d_in[8],
                                       (const u16*)d_in[9], (const u16*)d_in[10], (u16*)d_out, (char*)d_ws);
}
```

```cpp
#include <hip/hip_runtime.h>
#include <cstdint>

typedef unsigned short u16;
typedef unsigned long long u64;
using u32x4 = __attribute__((ext_vector_type(4))) unsigned;
using u32x3 = __attribute__((ext_vector_type(3))) unsigned;
using u32x2 = __attribute__((ext_vector_type(2))) unsigned;
using bf16x2 = __attribute__((ext_vector_type(2))) __bf16;
typedef __amdgpu_buffer_rsrc_t rsrc_t;

#define NP 4
#define NL 6
#define PF_DEPTH 6
#define POLL_A 2
#define POLL_B 1
#define STAG 37
#define KVAUX 2
#define KVMOD "nt"

#define WAUX 0
#define TNT 0
template <bool NTL> __device__ __forceinline__ u32x4 ldw(const void* p) {
  if (NTL) return __builtin_nontemporal_load((const u32x4*)p);
  return *(const u32x4*)p;
}
#define KAUX KVAUX
constexpr int D = 640, H = 32, SK = 32768, DH = 192, HD = H * DH, F = 1536, NMOD = 6 * D;
constexpr unsigned SENT = 0xFFFFFFFFu, OOB = 0x7ff00000u;
constexpr int NT = SK / 8;
constexpr int TILEB = 8 * DH * 2;
constexpr int NPS = (NP + NL) / 2;
#define XPF 1
constexpr int NSTAT = NPS * 64 + 2 * 128 + XPF * (NP / 2) * 48;
#define PREDPOLL 1
#define PIPE_A 0
#define PIPE_B 0
#define PIPE_C 0
#define DELAY_M 120
#define DELAY_M2 80
#define Q_SLEEP 0
#define DELAY_Q 32
#define POLL_Q 1
#define POLL_M 6
#define PRE_W0 1
#define PRE_SLEEP 2
#define DELAY_B 48
#define DELAY_C 12
#define POLL_C 1
#define XPF_DEPTH 6
constexpr int PW0 = XPF ? 1 : 4;
constexpr int VOL = 16;
constexpr unsigned SPIN_CAP = 1u << 18;
constexpr unsigned W_EPOCH = 0, W_MODG = 4096, W_RDY = W_MODG + 1920 * 8, W_QS = 32768, W_ML = 49152, W_CNT = 57344, W_YP = 65536, W_ACT = 90112,
                   W_PART = 131072, W_USED = W_PART + 512 * DH * 4;
constexpr unsigned W_STAMP = 1u << 20;

__device__ __forceinline__ float bf2f(unsigned v) { return __builtin_bit_cast(float, v << 16); }
__device__ __forceinline__ float rbf(float x) { return (float)(__bf16)x; }
__device__ __forceinline__ float lo(unsigned u) { return __builtin_bit_cast(float, u << 16); }
__device__ __forceinline__ float hi(unsigned u) { return __builtin_bit_cast(float, u & 0xffff0000u); }
__device__ __forceinline__ unsigned f2u(float x) { return __builtin_bit_cast(unsigned, x); }
__device__ __forceinline__ float u2f(unsigned x) { return __builtin_bit_cast(float, x); }
__device__ __forceinline__ unsigned pack(float a, float b) { return (f2u(a) >> 16) | (f2u(b) & 0xffff0000u); }
__device__ __forceinline__ unsigned canon(unsigned u) { return u == SENT ? 0x7fc07fc0u : u; }
__device__ __forceinline__ float dot2(unsigned k, unsigned q, float acc) {
  return __builtin_amdgcn_fdot2_f32_bf16(__builtin_bit_cast(bf16x2, k), __builtin_bit_cast(bf16x2, q), acc, false);
}
template <int CTRL> __device__ __forceinline__ float dpp(float x) {
  return u2f(__builtin_amdgcn_mov_dpp(f2u(x), CTRL, 0xf, 0xf, true));
}
constexpr int XOR1 = 0xB1, XOR2 = 0x4E, HMIR = 0x141, RMIR = 0x140, ROR8 = 0x128, SHL4 = 0x104;
__device__ __forceinline__ float silu(float x) { return x / (1.f + __expf(-x)); }
__device__ __forceinline__ float red32(float a, float b) {
  auto r = __builtin_amdgcn_permlane32_swap(f2u(a), f2u(b), false, false);
  return u2f(r[0]) + u2f(r[1]);
}
__device__ __forceinline__ float red16(float a, float b) {
  auto r = __builtin_amdgcn_permlane16_swap(f2u(a), f2u(b), false, false);
  return u2f(r[0]) + u2f(r[1]);
}
__device__ __forceinline__ float red8(float a, float b, bool bit3) {
  const float keep = bit3 ? b : a, send = bit3 ? a : b;
  return keep + dpp<ROR8>(send);
}
__device__ __forceinline__ float max32(float a) {
  auto r = __builtin_amdgcn_permlane32_swap(f2u(a), f2u(a), false, false);
  return fmaxf(u2f(r[0]), u2f(r[1]));
}
__device__ __forceinline__ float max16(float a) {
  auto r = __builtin_amdgcn_permlane16_swap(f2u(a), f2u(a), false, false);
  return fmaxf(u2f(r[0]), u2f(r[1]));
}
__device__ __forceinline__ float wave_sum(float v) {
  v += dpp<XOR1>(v); v += dpp<XOR2>(v); v += dpp<HMIR>(v); v += dpp<RMIR>(v);
  v = red16(v, v); v = red32(v, v);
  return v;
}
__device__ __forceinline__ float red_oct(const float (&a)[8], bool bit3) {
  const float b0 = red32(a[0], a[4]), b1 = red32(a[1], a[5]), b2 = red32(a[2], a[6]), b3 = red32(a[3], a[7]);
  const float c0 = red16(b0, b2), c1 = red16(b1, b3);
  return red8(c0, c1, bit3);
}
__device__ __forceinline__ void mac2(float (&acc)[8], const u32x4& w0, const u32x4& w1, unsigned ap) {
#pragma unroll
  for (int i = 0; i < 4; ++i) {
    acc[2 * i] = dot2(__builtin_amdgcn_perm(w1[i], w0[i], 0x05040100u), ap, acc[2 * i]);
    acc[2 * i + 1] = dot2(__builtin_amdgcn_perm(w1[i], w0[i], 0x07060302u), ap, acc[2 * i + 1]);
  }
}
__device__ __forceinline__ void preperm(unsigned (&wp)[24], const u32x4 (&w)[6]) {
#pragma unroll
  for (int u = 0; u < 3; ++u)
#pragma unroll
    for (int i = 0; i < 4; ++i) {
      wp[u * 8 + 2 * i] = __builtin_amdgcn_perm(w[2 * u + 1][i], w[2 * u][i], 0x05040100u);
      wp[u * 8 + 2 * i + 1] = __builtin_amdgcn_perm(w[2 * u + 1][i], w[2 * u][i], 0x07060302u);
    }
#pragma unroll
  for (int i = 0; i < 24; i += 8)
    asm volatile("" : "+v"(wp[i]), "+v"(wp[i + 1]), "+v"(wp[i + 2]), "+v"(wp[i + 3]), "+v"(wp[i + 4]), "+v"(wp[i + 5]), "+v"(wp[i + 6]), "+v"(wp[i + 7]));
}
__device__ __forceinline__ void macp(float (&acc)[8], const unsigned* wp, unsigned ap) {
#pragma unroll
  for (int i = 0; i < 8; ++i) acc[i] = dot2(wp[i], ap, acc[i]);
}
__device__ __forceinline__ float sum8(const float* p) {
  const float4 a = *(const float4*)p, b = *(const float4*)(p + 4);
  return ((a.x + a.y) + (a.z + a.w)) + ((b.x + b.y) + (b.z + b.w));
}
__device__ __forceinline__ bool valid4(const u32x4& v) { return v[0] != SENT && v[1] != SENT && v[2] != SENT && v[3] != SENT; }
__device__ __forceinline__ unsigned umax3(unsigned a, unsigned b, unsigned c) { return max(max(a, b), c); }
__device__ __forceinline__ unsigned umax4(const u32x4& v) { return umax3(v[0], v[1], max(v[2], v[3])); }
__device__ __forceinline__ u64 ld_gran(const char* ws, unsigned idx) {
  return __hip_atomic_load((const u64*)(ws + W_MODG) + idx, __ATOMIC_RELAXED, __HIP_MEMORY_SCOPE_AGENT);
}
__device__ __forceinline__ unsigned opq(unsigned& v) { asm volatile("" : "+v"(v)); return v; }
#define MEMFENCE() asm volatile("" ::: "memory")
#define WAIT_VM(n) __builtin_amdgcn_s_waitcnt(0x0F70 | ((n) & 15) | (((n) >> 4) << 14))
__device__ __forceinline__ void wg_barrier() {
  asm volatile("s_waitcnt lgkmcnt(0)\n\ts_barrier" ::: "memory");
}
__device__ __forceinline__ void lds_arrive(unsigned* cnt, int lane) {
  asm volatile("s_waitcnt lgkmcnt(0)" ::: "memory");
  if (lane == 0) __hip_atomic_fetch_add(cnt, 1u, __ATOMIC_RELAXED, __HIP_MEMORY_SCOPE_WORKGROUP);
  MEMFENCE();
}
__device__ __forceinline__ void lds_wait(unsigned* cnt, unsigned target) {
  MEMFENCE();
  _Pragma("nounroll") for (unsigned i = 0; i < (1u << 24); ++i) {
    if (__hip_atomic_load(cnt, __ATOMIC_RELAXED, __HIP_MEMORY_SCOPE_WORKGROUP) >= target) break;
    __builtin_amdgcn_s_sleep(1);
  }
  MEMFENCE();
}

struct Tile { u32x4 k[3], v[3]; };
__device__ __forceinline__ void ld_tile(Tile& t, rsrc_t rk, rsrc_t rv, unsigned voff, unsigned soff) {
#pragma unroll
  for (int j = 0; j < 3; ++j) t.k[j] = __builtin_amdgcn_raw_buffer_load_b128(rk, voff + j * 128, soff, KAUX);
#pragma unroll
  for (int j = 0; j < 3; ++j) t.v[j] = __builtin_amdgcn_raw_buffer_load_b128(rv, voff + j * 128, soff, KVAUX);
  MEMFENCE();
  __builtin_amdgcn_sched_barrier(0);
}
__device__ __forceinline__ void ld_tile_lds(unsigned ldst, rsrc_t rk, rsrc_t rv, unsigned v0, unsigned v1, unsigned v2, unsigned soff) {
  unsigned keep;
  asm volatile("s_mov_b32 %0, m0\n\t"
               "s_mov_b32 m0, %1\n\ts_nop 0\n\tbuffer_load_dwordx4 %2, %5, %7 offen " KVMOD " lds\n\t"
               "s_addk_i32 m0, 0x400\n\ts_nop 0\n\tbuffer_load_dwordx4 %3, %5, %7 offen " KVMOD " lds\n\t"
               "s_addk_i32 m0, 0x400\n\ts_nop 0\n\tbuffer_load_dwordx4 %4, %5, %7 offen " KVMOD " lds\n\t"
               "s_addk_i32 m0, 0x400\n\ts_nop 0\n\tbuffer_load_dwordx4 %2, %6, %7 offen " KVMOD " lds\n\t"
               "s_addk_i32 m0, 0x400\n\ts_nop 0\n\tbuffer_load_dwordx4 %3, %6, %7 offen " KVMOD " lds\n\t"
               "s_addk_i32 m0, 0x400\n\ts_nop 0\n\tbuffer_load_dwordx4 %4, %6, %7 offen " KVMOD " lds\n\t"
               "s_mov_b32 m0, %0"
               : "=&s"(keep) : "s"(ldst), "v"(v0), "v"(v1), "v"(v2), "s"(rk), "s"(rv), "s"(soff) : "memory", "scc");
}
__device__ __forceinline__ void use_tile(const Tile& t, const unsigned (&qp)[12], float& m, float& l, float (&acc)[24]) {
  float d = 0.f;
#pragma unroll
  for (int j = 0; j < 3; ++j)
#pragma unroll
    for (int i = 0; i < 4; ++i) d = dot2(t.k[j][i], qp[j * 4 + i], d);
  d += dpp<XOR1>(d); d += dpp<XOR2>(d); d += dpp<HMIR>(d);
  const float sc = rbf(rbf(d) * 0.07216878364870322f);
  const float mn = fmaxf(m, sc);
  const float alpha = __expf(m - mn), p = __expf(sc - mn);
  m = mn; l = l * alpha + p;
#pragma unroll
  for (int j = 0; j < 3; ++j)
#pragma unroll
    for (int i = 0; i < 4; ++i) {
      acc[j * 8 + 2 * i] = fmaf(p, lo(t.v[j][i]), acc[j * 8 + 2 * i] * alpha);
      acc[j * 8 + 2 * i + 1] = fmaf(p, hi(t.v[j][i]), acc[j * 8 + 2 * i + 1] * alpha);
    }
}

#define STAMP(i) do { } while (0)

__global__ void __launch_bounds__(512, 2)
fused_block(const u16* __restrict__ x, const u16* __restrict__ Kc, const u16* __restrict__ Vc, const u16* __restrict__ wq,
            const u16* __restrict__ wo, const u16* __restrict__ wg, const u16* __restrict__ wu, const u16* __restrict__ wd,
            const u16* __restrict__ cvec, const u16* __restrict__ wmod, u16* __restrict__ out, char* ws) {
  __shared__ __attribute__((aligned(16))) unsigned s_flag[8];
  __shared__ __attribute__((aligned(16))) unsigned s_hp[384];
  __shared__ __attribute__((aligned(16))) float s_red[8][32];
  __shared__ __attribute__((aligned(16))) unsigned s_q[192];
  __shared__ __attribute__((aligned(16))) unsigned s_big[(NL > 2 ? NL : 2) * 4 * 1536];
  __shared__ __attribute__((aligned(16))) float s_ml[16][8];
  __shared__ __attribute__((aligned(16))) float s_x1[D];

  const int tid = threadIdx.x, lane = tid & 63, wave = __builtin_amdgcn_readfirstlane(tid >> 6);
  const int b = blockIdx.x, s = b & 7, h = b >> 3;
  const int h0 = h & 15, h1 = h0 + 16, w16 = (h >> 4) * 8 + s;
  float (*s_mrg)[4 * DH] = (float (*)[4 * DH])s_big;
  float (*s_rt)[8] = (float (*)[8])s_red;
  const bool bit3 = (lane & 8) != 0;
  const rsrc_t rw = __builtin_amdgcn_make_buffer_rsrc(ws, 0, W_USED, 0x00020000);
  const rsrc_t rk0 = __builtin_amdgcn_make_buffer_rsrc((char*)Kc + (size_t)h0 * SK * DH * 2, 0, SK * DH * 2, 0x00020000);
  const rsrc_t rv0 = __builtin_amdgcn_make_buffer_rsrc((char*)Vc + (size_t)h0 * SK * DH * 2, 0, SK * DH * 2, 0x00020000);
  const rsrc_t rk1 = __builtin_amdgcn_make_buffer_rsrc((char*)Kc + (size_t)h1 * SK * DH * 2, 0, SK * DH * 2, 0x00020000);
  const rsrc_t rv1 = __builtin_amdgcn_make_buffer_rsrc((char*)Vc + (size_t)h1 * SK * DH * 2, 0, SK * DH * 2, 0x00020000);
  const unsigned voff = (lane >> 3) * 384 + (lane & 7) * 16;
  const bool roleA = s < 5, qprod = s < 6;
  const int ag = (s * 32 + h) / 20, ak = (s * 32 + h) % 20;
  const int bc = (s - 5) * 32 + h;
  const int cc = bc;
  const bool roleC = s >= 5 && cc < 40;
  const int qc = s * 32 + h;
  const int cg = tid & 3, rs = tid >> 2;
  const int pb = w16 * 4 + (wave - 4), gb = NPS * 64 + w16 * 8 + wave;
  auto so0 = [&](int idx) -> unsigned { return (unsigned)(((idx + h0 * STAG) & (NT - 1)) * TILEB); };
  auto so1 = [&](int idx) -> unsigned { return (unsigned)(((idx + h1 * STAG) & (NT - 1)) * TILEB); };

  STAMP(0);
  Tile A, B, P[NP];
  const unsigned ep = __hip_atomic_load((const unsigned*)(ws + W_EPOCH), __ATOMIC_RELAXED, __HIP_MEMORY_SCOPE_AGENT);
  unsigned tag;
  if (wave >= 4) {
    if (wave == 4) {
      const u32x4 s4 = {SENT, SENT, SENT, SENT};
      __builtin_amdgcn_raw_buffer_store_b128(s4, rw, lane < 48 ? W_PART + (h0 * 16 + w16) * (DH * 4) + lane * 16 : OOB, 0, 16);
      __builtin_amdgcn_raw_buffer_store_b128(s4, rw, lane < 48 ? W_PART + (h1 * 16 + w16) * (DH * 4) + lane * 16 : OOB, 0, 16);
      unsigned off = OOB, val = SENT;
      if (lane < 16) { if (qprod) off = W_QS + (qc * 16 + lane) * 4; }
      else if (lane < 48) { if (roleA) off = W_YP + (ag * D + ak * 32 + (lane - 16)) * 4; }
      else if (lane < 56) { if (!roleA) off = W_ACT + (bc * 8 + (lane - 48)) * 4; }
      else if (lane < 60) off = W_ML + ((lane & 2) ? h1 : h0) * 128 + w16 * 8 + (lane & 1) * 4;
      else if (lane == 60) { if (w16 == 0) { off = W_CNT + h0 * 256; val = 0; } }
      __builtin_amdgcn_raw_buffer_store_b32(val, rw, off, 0, 16);
      if (lane < 8) s_flag[lane] = 0;
    }
    wg_barrier();
    tag = ep * 1664525u + 1013904223u;
    if (wave == 4) {
      asm volatile("s_waitcnt vmcnt(0)" ::: "memory");
      if (lane == 0) __hip_atomic_store((u64*)(ws + W_RDY) + b, (u64)tag << 32, __ATOMIC_RELAXED, __HIP_MEMORY_SCOPE_AGENT);
    }
#pragma unroll
    for (int i = 0; i < NP; ++i) {
      if (i & 1) ld_tile(P[i], rk1, rv1, voff, so1(pb + 64 * (i >> 1))); else ld_tile(P[i], rk0, rv0, voff, so0(pb + 64 * (i >> 1)));
      if (i) WAIT_VM(6);
    }
    {
      const unsigned lbase = (unsigned)(uintptr_t)(__attribute__((address_space(3))) unsigned*)s_big + (wave - 4) * (NL * 6144);
#pragma unroll
      for (int i = 0; i < NL; ++i) {
        const int st = pb + 64 * ((NP + i) >> 1);
        if (i & 1) ld_tile_lds(lbase + i * 6144, rk1, rv1, voff, voff + 128, voff + 256, so1(st));
        else ld_tile_lds(lbase + i * 6144, rk0, rv0, voff, voff + 128, voff + 256, so0(st));
        WAIT_VM(6);
      }
    }
    ld_tile(A, rk0, rv0, voff, so0(gb));
    WAIT_VM(6);
    ld_tile(B, rk1, rv1, voff, so1(gb));
    STAMP(1);
  } else {
    const rsrc_t ru = qprod ? __builtin_amdgcn_make_buffer_rsrc((void*)wq, 0, D * HD * 2, 0x00020000)
                            : __builtin_amdgcn_make_buffer_rsrc((void*)wmod, 0, D * NMOD * 2, 0x00020000);
    const unsigned rstride = qprod ? HD * 2 : NMOD * 2;
    const int mu = (s - 6) * 32 + h;
    u32x4 wt[10];
    unsigned av[5];
    {
      const rsrc_t ra = __builtin_amdgcn_make_buffer_rsrc((void*)(qprod ? x : cvec), 0, D * 2, 0x00020000);
      const unsigned a0 = qprod ? lane * 4 : rs * 20, as = qprod ? 256 : 4;
#pragma unroll
      for (int i = 0; i < 5; ++i) av[i] = __builtin_amdgcn_raw_buffer_load_b32(ra, a0 + as * i, 0, 0);
      const unsigned so = qprod ? qc * 64 : mu * 64;
      if (Q_SLEEP && qprod) { MEMFENCE(); __builtin_amdgcn_s_sleep(Q_SLEEP); MEMFENCE(); }
#pragma unroll
      for (int i = 0; i < 10; ++i) wt[i] = __builtin_amdgcn_raw_buffer_load_b128(ru, rs * 10 * rstride + cg * 16, so + i * rstride, WAUX);
    }
    MEMFENCE();
    wg_barrier();
    tag = ep * 1664525u + 1013904223u;
    unsigned arrivals = 0;
    auto gemv = [&](const unsigned (&ap)[5]) -> float {
      float acc[8];
#pragma unroll
      for (int i = 0; i < 8; ++i) acc[i] = 0.f;
#pragma unroll
      for (int u = 0; u < 5; ++u) mac2(acc, wt[2 * u], wt[2 * u + 1], ap[u]);
      float v = red_oct(acc, bit3);
      v += dpp<SHL4>(v);
      float* red = &s_red[(arrivals & 4) + wave][0];
      if ((lane & 4) == 0) red[cg * 8 + (lane >> 3)] = v;
      lds_arrive(&s_flag[1], lane);
      float r = 0.f;
      if (wave == 0) {
        lds_wait(&s_flag[1], arrivals + 4);
        const int cI = lane & 31, o = arrivals & 4;
        r = rbf((s_red[o][cI] + s_red[o + 1][cI]) + (s_red[o + 2][cI] + s_red[o + 3][cI]));
      }
      arrivals += 4;
      return r;
    };
    float n0 = 0.f, n1 = 0.f, n2 = 0.f, n3 = 0.f;
    if (!qprod) {
      unsigned ap[5];
#pragma unroll
      for (int u = 0; u < 5; ++u) ap[u] = pack(rbf(silu(lo(av[u]))), rbf(silu(hi(av[u]))));
#pragma nounroll
      for (int rnd = 0; rnd < 2; ++rnd) {
        const int unit = rnd * 64 + mu;
        const float r = gemv(ap);
        if (rnd == 0) {
          const unsigned so = mu + 64 < 120 ? (mu + 64) * 64 : OOB;
#pragma unroll
          for (int i = 0; i < 10; ++i) wt[i] = __builtin_amdgcn_raw_buffer_load_b128(ru, rs * 10 * rstride + cg * 16, so + i * rstride, WAUX);
        }
        const unsigned pr = pack(r, dpp<XOR1>(r));
        if (wave == 0 && lane < 32 && (lane & 1) == 0 && unit < 120)
          __hip_atomic_store((u64*)(ws + W_MODG) + unit * 16 + (lane >> 1), ((u64)tag << 32) | pr, __ATOMIC_RELAXED, __HIP_MEMORY_SCOPE_AGENT);
        if (rnd == 0) STAMP(1);
      }
    } else {
      float sum = 0.f;
#pragma unroll
      for (int i = 0; i < 5; ++i) sum += lo(av[i]) + hi(av[i]);
      const float mean = wave_sum(sum) * (1.f / D);
      float sq = 0.f;
#pragma unroll
      for (int i = 0; i < 5; ++i) { const float a = lo(av[i]) - mean, c2 = hi(av[i]) - mean; sq += a * a + c2 * c2; }
      const float var = wave_sum(sq) * (1.f / D);
      const float mb = rbf(mean), den = rbf(sqrtf(rbf(rbf(var) + 1e-5f)));
      const unsigned xa = wave == 0 ? av[0] : wave == 1 ? av[1] : wave == 2 ? av[2] : av[3];
      n0 = rbf(rbf(lo(xa) - mb) / den); n1 = rbf(rbf(hi(xa) - mb) / den);
      n2 = rbf(rbf(lo(av[4]) - mb) / den); n3 = rbf(rbf(hi(av[4]) - mb) / den);
      STAMP(1);
    }
    {
      const unsigned i0 = tid, i1 = 320 + tid, i2 = 1920 + tid;
      const unsigned i3 = wave == 0 ? 256 + tid : i0, i4 = wave == 0 ? 576 + tid : i1;
      u64 g0, g1, g2, g3, g4;
      if (qprod) { __builtin_amdgcn_s_sleep(DELAY_M); __builtin_amdgcn_s_sleep(DELAY_M2); }
      _Pragma("nounroll") for (unsigned it = 0; it < SPIN_CAP; ++it) {
        g0 = ld_gran(ws, i0); g1 = ld_gran(ws, i1); g2 = ld_gran(ws, i2); g3 = ld_gran(ws, i3); g4 = ld_gran(ws, i4);
        const bool ok = (unsigned)(g0 >> 32) == tag && (unsigned)(g1 >> 32) == tag && (unsigned)(g2 >> 32) == tag &&
                        (unsigned)(g3 >> 32) == tag && (unsigned)(g4 >> 32) == tag;
        if (__all(ok)) break;
        __builtin_amdgcn_s_sleep(POLL_M);
      }
      const unsigned sa = (unsigned)g0, ca = (unsigned)g1;
      s_hp[tid] = pack(rbf(rbf(n0 * rbf(1.f + lo(ca))) + lo(sa)), rbf(rbf(n1 * rbf(1.f + hi(ca))) + hi(sa)));
      if (wave == 0) {
        const unsigned sa2 = (unsigned)g3, ca2 = (unsigned)g4;
        s_hp[256 + tid] = pack(rbf(rbf(n2 * rbf(1.f + lo(ca2))) + lo(sa2)), rbf(rbf(n3 * rbf(1.f + hi(ca2))) + hi(sa2)));
      }
    }
    STAMP(2);
    lds_arrive(&s_flag[0], lane);
    if (qprod || wave == 0) lds_wait(&s_flag[0], 4);
    if (qprod) {
      unsigned ap[5];
#pragma unroll
      for (int u = 0; u < 5; ++u) ap[u] = s_hp[rs * 5 + u];
      const float r = gemv(ap);
      const unsigned pr = canon(pack(r, dpp<XOR1>(r)));
      if (wave == 0)
        __builtin_amdgcn_raw_buffer_store_b32(pr, rw, (lane < 32 && (lane & 1) == 0) ? W_QS + (qc * 16 + (lane >> 1)) * 4 : OOB, 0, 16);
    }
    STAMP(3);
    if (wave == 0) {
      u32x4 qv;
      __builtin_amdgcn_s_sleep(DELAY_Q);
      _Pragma("nounroll") for (unsigned it = 0; it < SPIN_CAP; ++it) {
        MEMFENCE();
        qv = __builtin_amdgcn_raw_buffer_load_b128(rw, lane < 24 ? W_QS + h0 * 384 + lane * 16 : lane < 48 ? W_QS + h1 * 384 + (lane - 24) * 16 : OOB, 0, 16);
        if (__all(umax4(qv) != SENT)) break;
        __builtin_amdgcn_s_sleep(POLL_Q);
      }
      if (lane < 48) *(u32x4*)&s_q[lane * 4] = qv;
      lds_arrive(&s_flag[2], lane);
    }
    if (wave != 0) {
      const int xb = NPS * 64 + 256 + w16 * 3 + (wave - 1);
#pragma unroll
      for (int i = 0; i < NP; ++i) {
        if (i & 1) ld_tile(P[i], rk1, rv1, voff, so1(xb + 48 * (i >> 1))); else ld_tile(P[i], rk0, rv0, voff, so0(xb + 48 * (i >> 1)));
        if (i) WAIT_VM(XPF_DEPTH);
      }
    }
    ld_tile(A, rk0, rv0, voff, so0(gb));
    if (wave != 0) WAIT_VM(XPF_DEPTH);
    ld_tile(B, rk1, rv1, voff, so1(gb));
  }
  lds_wait(&s_flag[2], 1);
  STAMP(4);
  unsigned q0[12], q1[12];
#pragma unroll
  for (int j = 0; j < 3; ++j) {
    const u32x4 t0 = *(const u32x4*)&s_q[j * 32 + (lane & 7) * 4], t1 = *(const u32x4*)&s_q[96 + j * 32 + (lane & 7) * 4];
#pragma unroll
    for (int i = 0; i < 4; ++i) { q0[j * 4 + i] = t0[i]; q1[j * 4 + i] = t1[i]; }
  }
  float m0 = -INFINITY, l0 = 0.f, acc0[24], m1 = -INFINITY, l1 = 0.f, acc1[24];
#pragma unroll
  for (int i = 0; i < 24; ++i) { acc0[i] = 0.f; acc1[i] = 0.f; }
  const unsigned coff = lane == 0 ? W_CNT + h0 * 256 : OOB;
  unsigned pend = (unsigned)__builtin_amdgcn_raw_ptr_buffer_atomic_add_i32(1, rw, coff, 0, 0);
  if (wave >= PW0) { use_tile(P[0], q0, m0, l0, acc0); use_tile(P[1], q1, m1, l1, acc1); }
  MEMFENCE();
  __builtin_amdgcn_sched_barrier(0);
  use_tile(A, q0, m0, l0, acc0);
  ld_tile(A, rk0, rv0, voff, so0(gb + 128));
  use_tile(B, q1, m1, l1, acc1);
  ld_tile(B, rk1, rv1, voff, so1(gb + 128));
  if (wave >= PW0) {
#pragma unroll
    for (int i = 2; i < NP; ++i) { if (i & 1) use_tile(P[i], q1, m1, l1, acc1); else use_tile(P[i], q0, m0, l0, acc0); }
  }
  if (wave >= 4) {
    const unsigned* lb = s_big + (wave - 4) * (NL * 1536) + lane * 4;
#pragma unroll
    for (int i = 0; i < NL; ++i) {
      Tile T;
#pragma unroll
      for (int j = 0; j < 3; ++j) { T.k[j] = *(const u32x4*)(lb + i * 1536 + j * 256); T.v[j] = *(const u32x4*)(lb + i * 1536 + 768 + j * 256); }
      if (i & 1) use_tile(T, q1, m1, l1, acc1); else use_tile(T, q0, m0, l0, acc0);
    }
  }
  if (wave >= 4) lds_arrive(&s_flag[4], lane);
  u32x4 wa[6], wc[6];
  const int bp0 = rs < 64 ? rs * 3 : 192 + (rs - 64) * 2;
  {
    if (roleA) {
#pragma unroll
      for (int i = 0; i < 6; ++i) wa[i] = ldw<(TNT & 1) != 0>(wo + (size_t)(ag * 768 + rs * 6 + i) * D + ak * 32 + cg * 8);
    } else {
      const u16* wgu = ((cg & 2) ? wu : wg) + (size_t)bp0 * 2 * F + bc * 16 + (cg & 1) * 8;
#pragma unroll
      for (int i = 0; i < 4; ++i) wa[i] = ldw<(TNT & 2) != 0>(wgu + i * F);
      const int ex = wave < 4 ? 4 : 0;
      wa[4] = ldw<(TNT & 2) != 0>(wgu + ex * F);
      wa[5] = ldw<(TNT & 2) != 0>(wgu + (ex + 1) * F);
    }
    if (roleC) {
#pragma unroll
      for (int i = 0; i < 6; ++i) wc[i] = ldw<(TNT & 4) != 0>(wd + (size_t)((tid >> 1) * 6 + i) * D + cc * 16 + (tid & 1) * 8);
    }
    MEMFENCE();
    __builtin_amdgcn_sched_barrier(0);
  }
  STAMP(5);
  while (true) {
    const int tn = NSTAT + __builtin_amdgcn_readfirstlane(pend);
    if (tn >= NT) break;
    const unsigned o0 = so0(tn), o1 = so1(tn);
    use_tile(A, q0, m0, l0, acc0);
    MEMFENCE();
    __builtin_amdgcn_sched_barrier(0);
    pend = (unsigned)__builtin_amdgcn_raw_ptr_buffer_atomic_add_i32(1, rw, coff, 0, 0);
    ld_tile(A, rk0, rv0, voff, o0);
    use_tile(B, q1, m1, l1, acc1);
    ld_tile(B, rk1, rv1, voff, o1);
  }
  lds_wait(&s_flag[4], 4);
  unsigned poff = OOB;
  {
    const int t = tid & 255, hd = (wave >> 2) ? h1 : h0;
    if (t < DH) poff = W_PART + ((hd * 16 + w16) * DH + t) * 4;
    else if (t < DH + 2) poff = W_ML + hd * 128 + w16 * 8 + (t - DH) * 4;
    asm volatile("" : "+v"(poff));
  }
  auto wave_merge = [&](float m, float l, const float (&acc)[24], int slot) {
    float M = fmaxf(m, dpp<ROR8>(m)); M = max16(M); M = max32(M);
    const float w = __expf(m - M);
    l *= w;
    l += dpp<ROR8>(l); l = red16(l, l); l = red32(l, l);
    float r12[12], r6[6];
#pragma unroll
    for (int i = 0; i < 12; ++i) r12[i] = red32(acc[i] * w, acc[i + 12] * w);
#pragma unroll
    for (int i = 0; i < 6; ++i) r6[i] = red16(r12[i], r12[i + 6]);
    const int ebase = 12 * ((lane >> 5) & 1) + 6 * ((lane >> 4) & 1) + 3 * ((lane >> 3) & 1);
#pragma unroll
    for (int i = 0; i < 3; ++i) {
      const float v = red8(r6[i], r6[i + 3], bit3);
      const int e = ebase + i;
      s_mrg[wave][slot * 256 + (e >> 3) * 64 + (lane & 7) * 8 + (e & 7)] = v;
    }
    if (lane == 0) { s_ml[slot][wave] = M; s_mrg[wave][slot * 256 + DH + 1] = l; }
  };
  use_tile(A, q0, m0, l0, acc0);
  wave_merge(m0, l0, acc0, 0);
  MEMFENCE();
  __builtin_amdgcn_sched_barrier(0);
  use_tile(B, q1, m1, l1, acc1);
  STAMP(6);
  wave_merge(m1, l1, acc1, 1);
  wg_barrier();
  {
    const float4 ma = *(const float4*)&s_ml[wave >> 2][0], mb4 = *(const float4*)&s_ml[wave >> 2][4];
    float r[8];
#pragma unroll
    for (int w = 0; w < 8; ++w) r[w] = s_mrg[w][tid];
    __builtin_amdgcn_sched_barrier(0);
    const float M = fmaxf(fmaxf(fmaxf(ma.x, ma.y), fmaxf(ma.z, ma.w)), fmaxf(fmaxf(mb4.x, mb4.y), fmaxf(mb4.z, mb4.w)));
    float o = __expf(ma.x - M) * r[0], o2 = __expf(mb4.x - M) * r[4];
    o = fmaf(__expf(ma.y - M), r[1], o); o2 = fmaf(__expf(mb4.y - M), r[5], o2);
    o = fmaf(__expf(ma.z - M), r[2], o); o2 = fmaf(__expf(mb4.z - M), r[6], o2);
    o = fmaf(__expf(ma.w - M), r[3], o); o2 = fmaf(__expf(mb4.w - M), r[7], o2);
    const float val = (tid & 255) == DH ? M : o + o2;
    __builtin_amdgcn_raw_buffer_store_b32(canon(f2u(val)), rw, poff, 0, 16);
  }
  STAMP(7);
  if (roleA) {
    unsigned wp[24];
    preperm(wp, wa);
    unsigned yoff = lane < 32 ? W_YP + (ag * D + ak * 32 + lane) * 4 : OOB;
    asm volatile("" : "+v"(yoff));
    wg_barrier();
    {
      unsigned po[3], bi[3];
#pragma unroll
      for (int i = 0; i < 3; ++i) {
        const int qd = lane + 64 * i, hh = qd / 48;
        po[i] = W_PART + (((ag * 4 + hh) * 16 + wave) * DH + (qd - hh * 48) * 4) * 4;
        bi[i] = hh * 16 + wave;
      }
      unsigned mo = W_ML + ag * 512 + lane * 8;
      u32x4 a[6]; u32x2 ml = {0u, 0u};
      bool pv = false, mv = wave != 0;
      _Pragma("nounroll") for (unsigned it = 0; it < SPIN_CAP; ++it) {
        MEMFENCE();
        if (!mv) ml = __builtin_amdgcn_raw_buffer_load_b64(rw, opq(mo), 0, VOL);
        if (!pv) {
          opq(po[0]); opq(po[1]); opq(po[2]);
#pragma unroll
          for (int i = 0; i < 6; ++i) a[i] = __builtin_amdgcn_raw_buffer_load_b128(rw, po[i % 3], (i / 3) * (8 * DH * 4), VOL);
          pv = __all(umax3(umax3(umax4(a[0]), umax4(a[1]), umax4(a[2])), umax4(a[3]), max(umax4(a[4]), umax4(a[5]))) != SENT);
        }
        mv = __all(max(ml[0], ml[1]) != SENT);
        if (pv && mv) break;
        __builtin_amdgcn_s_sleep(POLL_A);
      }
      if (wave == 0) {
        const float mi = u2f(ml[0]);
        float M = fmaxf(mi, dpp<XOR1>(mi)); M = fmaxf(M, dpp<XOR2>(M)); M = fmaxf(M, dpp<HMIR>(M)); M = fmaxf(M, dpp<RMIR>(M));
        const float e = __expf(mi - M);
        float L = e * u2f(ml[1]);
        L += dpp<XOR1>(L); L += dpp<XOR2>(L); L += dpp<HMIR>(L); L += dpp<RMIR>(L);
        s_x1[lane] = e * __builtin_amdgcn_rcpf(L);
        lds_arrive(&s_flag[5], lane);
      } else {
        lds_wait(&s_flag[5], 1);
      }
      STAMP(8);
#pragma unroll
      for (int i = 0; i < 3; ++i) {
        const float f0 = s_x1[bi[i]], f1 = s_x1[bi[i] + 8];
        float4 t;
        t.x = fmaf(u2f(a[i + 3][0]), f1, u2f(a[i][0]) * f0); t.y = fmaf(u2f(a[i + 3][1]), f1, u2f(a[i][1]) * f0);
        t.z = fmaf(u2f(a[i + 3][2]), f1, u2f(a[i][2]) * f0); t.w = fmaf(u2f(a[i + 3][3]), f1, u2f(a[i][3]) * f0);
        *(float4*)&s_mrg[wave][i * 256 + lane * 4] = t;
      }
    }
    wg_barrier();
    if (tid < 384) {
      float2 r[8];
#pragma unroll
      for (int i = 0; i < 8; ++i) r[i] = *(const float2*)&s_mrg[i][tid * 2];
      const float o0 = ((r[0].x + r[1].x) + (r[2].x + r[3].x)) + ((r[4].x + r[5].x) + (r[6].x + r[7].x));
      const float o1 = ((r[0].y + r[1].y) + (r[2].y + r[3].y)) + ((r[4].y + r[5].y) + (r[6].y + r[7].y));
      s_hp[tid] = pack(rbf(o0), rbf(o1));
    }
    wg_barrier();
    float ac[8];
#pragma unroll
    for (int i = 0; i < 8; ++i) ac[i] = 0.f;
#pragma unroll
    for (int u = 0; u < 3; ++u) macp(ac, wp + 8 * u, s_hp[rs * 3 + u]);
    float v = red_oct(ac, bit3);
    v += dpp<SHL4>(v);
    if ((lane & 4) == 0) s_rt[cg * 8 + (lane >> 3)][wave] = v;
    wg_barrier();
    if (wave == 0) {
      const float y = sum8(&s_rt[lane & 31][0]);
      __builtin_amdgcn_raw_buffer_store_b32(canon(f2u(y)), rw, yoff, 0, 16);
    }
    STAMP(9);
  } else {
    const int t2 = tid < 320 ? tid : 0;
    unsigned wp[24], wq2[24];
    preperm(wp, wa);
    if (roleC) preperm(wq2, wc);
    unsigned gga, gsb, gcb, ggb;
    const unsigned xpair = ((const unsigned*)x)[t2];
    {
      u64 g0, g1, g2, g3;
      const unsigned i3 = 1600 + (roleC ? cc * 8 : 0) + ((lane & 15) >> 1);
      _Pragma("nounroll") for (unsigned it = 0; it < SPIN_CAP; ++it) {
        g0 = ld_gran(ws, 640 + t2); g1 = ld_gran(ws, 960 + t2); g2 = ld_gran(ws, 1280 + t2); g3 = ld_gran(ws, i3);
        if (__all((unsigned)(g0 >> 32) == tag && (unsigned)(g1 >> 32) == tag && (unsigned)(g2 >> 32) == tag && (unsigned)(g3 >> 32) == tag)) break;
        __builtin_amdgcn_s_sleep(2);
      }
      gga = (unsigned)g0; gsb = (unsigned)g1; gcb = (unsigned)g2; ggb = (unsigned)g3;
    }
    float c1a = rbf(1.f + lo(gcb)), c1b = rbf(1.f + hi(gcb));
    unsigned aoff = (lane < 16 && (lane & 1) == 0) ? W_ACT + (bc * 8 + (lane >> 1)) * 4 : OOB;
    asm volatile("" : "+v"(c1a), "+v"(c1b), "+v"(aoff));
    float xa = 0.f, xb = 0.f;
    u32x2 ya[8], yb[8];
#pragma unroll
    for (int i = 0; i < 8; ++i) { ya[i] = u32x2{0u, 0u}; yb[i] = u32x2{0u, 0u}; }
    if (wave < 5) {
      if (wave == 0 || !PRE_W0) {
        _Pragma("nounroll") for (unsigned it = 0; it < SPIN_CAP; ++it) {
          MEMFENCE();
          const u32x4 t0 = __builtin_amdgcn_raw_buffer_load_b128(rw, W_ML + lane * 32, 0, 16);
          const u32x4 t1 = __builtin_amdgcn_raw_buffer_load_b128(rw, W_ML + lane * 32 + 16, 0, 16);
          if (__all(max(umax4(t0), umax4(t1)) != SENT)) break;
          __builtin_amdgcn_s_sleep(PRE_SLEEP);
        }
        if (PRE_W0) lds_arrive(&s_flag[5], lane);
      } else {
        lds_wait(&s_flag[5], 1);
      }
      __builtin_amdgcn_s_sleep(DELAY_B);
      float y0 = 0.f, y1 = 0.f;
      unsigned yo = W_YP + t2 * 8;
      auto y_issue = [&](u32x2 (&d)[8]) {
        opq(yo);
#pragma unroll
        for (int i = 0; i < 8; ++i) d[i] = __builtin_amdgcn_raw_buffer_load_b64(rw, yo, i * (D * 4), VOL);
        MEMFENCE();
      };
      auto y_take = [&](const u32x2 (&d)[8]) -> bool {
        unsigned mx = 0;
        y0 = 0.f; y1 = 0.f;
#pragma unroll
        for (int i = 0; i < 8; ++i) { mx = umax3(mx, d[i][0], d[i][1]); y0 += u2f(d[i][0]); y1 += u2f(d[i][1]); }
        return __all(mx != SENT);
      };
      _Pragma("nounroll") for (unsigned it = 0; it < SPIN_CAP; ++it) {
        y_issue(ya);
        if (y_take(ya)) break;
        __builtin_amdgcn_s_sleep(POLL_B);
      }
      xa = rbf(lo(xpair) + rbf(lo(gga) * rbf(y0)));
      xb = rbf(hi(xpair) + rbf(hi(gga) * rbf(y1)));
      const float k = u2f(__builtin_amdgcn_readfirstlane(f2u(xa)));
      const float da = xa - k, db = xb - k;
      const float S = wave_sum(da + db), Q = wave_sum(fmaf(da, da, db * db));
      *(float2*)&s_x1[tid * 2] = make_float2(xa, xb);
      if (lane == 0) *(float2*)&s_ml[4][wave * 2] = make_float2(k + S * (1.f / 128), Q - S * S * (1.f / 128));
    }
    STAMP(8);
    wg_barrier();
    if (wave < 5) {
      const float4 s0 = *(const float4*)&s_ml[4][0], s1 = *(const float4*)&s_ml[4][4];
      const float2 s2 = *(const float2*)&s_ml[5][0];
      __builtin_amdgcn_sched_barrier(0);
      const float mean = (((s0.x + s0.z) + (s1.x + s1.z)) + s2.x) * 0.2f;
      const float d0 = s0.x - mean, d1 = s0.z - mean, d2 = s1.x - mean, d3 = s1.z - mean, d4 = s2.x - mean;
      const float M2 = fmaf(128.f, fmaf(d0, d0, d1 * d1) + fmaf(d2, d2, fmaf(d3, d3, d4 * d4)), ((s0.y + s0.w) + (s1.y + s1.w)) + s2.y);
      const float mb = rbf(mean), den = rbf(__builtin_amdgcn_sqrtf(rbf(rbf(M2 * (1.f / D)) + 1e-5f)));
      const float rden = __builtin_amdgcn_rcpf(den);
      const float na = rbf(rbf(xa - mb) * rden), nb = rbf(rbf(xb - mb) * rden);
      s_hp[tid] = pack(rbf(rbf(na * c1a) + lo(gsb)), rbf(rbf(nb * c1b) + hi(gsb)));
    }
    wg_barrier();
    float ac[8];
#pragma unroll
    for (int i = 0; i < 8; ++i) ac[i] = 0.f;
    macp(ac, wp, s_hp[bp0]);
    macp(ac, wp + 8, s_hp[bp0 + 1]);
    if (wave < 4) macp(ac, wp + 16, s_hp[bp0 + 2]);
    float v = red_oct(ac, bit3);
    v += dpp<SHL4>(v);
    if ((lane & 4) == 0) s_rt[cg * 8 + (lane >> 3)][wave] = v;
    wg_barrier();
    if (wave == 0) {
      const unsigned gu = f2u(rbf(sum8(&s_rt[lane & 31][0])));
      const auto sw = __builtin_amdgcn_permlane16_swap(gu, gu, false, false);
      const float g = u2f(sw[0]), u = u2f(sw[1]);
      const float av = rbf(rbf(g * __builtin_amdgcn_rcpf(1.f + __expf(-g))) * u);
      const unsigned pr = canon(pack(av, dpp<XOR1>(av)));
      __builtin_amdgcn_raw_buffer_store_b32(pr, rw, aoff, 0, 16);
    }
    STAMP(9);
    if (roleC) {
      u32x3 ap;
      float x1v = s_x1[cc * 16 + (lane & 15)], gbv = (lane & 1) ? hi(ggb) : lo(ggb);
      u16* op = out + cc * 16 + (lane & 15);
      asm volatile("" : "+v"(x1v), "+v"(gbv), "+v"(op));
      __builtin_amdgcn_s_sleep(DELAY_C);
      _Pragma("nounroll") for (unsigned it = 0; it < SPIN_CAP; ++it) {
        MEMFENCE();
        ap = __builtin_amdgcn_raw_buffer_load_b96(rw, W_ACT + (tid >> 1) * 12, 0, 16);
        if (__all(umax3(ap[0], ap[1], ap[2]) != SENT)) break;
        __builtin_amdgcn_s_sleep(POLL_C);
      }
      STAMP(10);
      float dc[8];
#pragma unroll
      for (int i = 0; i < 8; ++i) dc[i] = 0.f;
#pragma unroll
      for (int u = 0; u < 3; ++u) macp(dc, wq2 + 8 * u, ap[u]);
      float dv = red_oct(dc, bit3);
      dv += dpp<SHL4>(dv); dv += dpp<XOR2>(dv);
      if ((lane & 6) == 0) s_ml[(lane & 1) * 8 + (lane >> 3)][wave] = dv;
      wg_barrier();
      if (tid < 16) {
        const float dn = sum8(&s_ml[tid][0]);
        const float r = x1v + rbf(gbv * rbf(dn));
        *op = (u16)(f2u(rbf(r)) >> 16);
        if (cc == 0 && tid == 0) __hip_atomic_store((unsigned*)(ws + W_EPOCH), tag, __ATOMIC_RELAXED, __HIP_MEMORY_SCOPE_AGENT);
      }
      STAMP(11);
    }
  }
}

extern "C" void kernel_launch(void* const* d_in, const int* in_sizes, int n_in, void* d_out, int out_size, void* d_ws, size_t ws_size,
                              hipStream_t stream) {
  fused_block<<<256, 512, 0, stream>>>((const u16*)d_in[0], (const u16*)d_in[1], (const u16*)d_in[2], (const u16*)d_in[3],
                                       (const u16*)d_in[4], (const u16*)d_in[6], (const u16*)d_in[7], (const u16*)d_in[8],
                                       (const u16*)d_in[9], (const u16*)d_in[10], (u16*)d_out, (char*)d_ws);
}
```

```cpp
#include <hip/hip_runtime.h>
#include <cstdint>

typedef unsigned short u16;
typedef unsigned long long u64;
using u32x4 = __attribute__((ext_vector_type(4))) unsigned;
using u32x3 = __attribute__((ext_vector_type(3))) unsigned;
using u32x2 = __attribute__((ext_vector_type(2))) unsigned;
using bf16x2 = __attribute__((ext_vector_type(2))) __bf16;
typedef __amdgpu_buffer_rsrc_t rsrc_t;

#define NP 4
#define NL 6
#define POLL_A 2
#define POLL_B 1
#define STAG 37
#define KVAUX 2
#define KVMOD "nt"

#define WAUX 2
#define TNT 0
template <bool NTL> __device__ __forceinline__ u32x4 ldw(const void* p) {
  if (NTL) return __builtin_nontemporal_load((const u32x4*)p);
  return *(const u32x4*)p;
}
#define KAUX KVAUX
constexpr int D = 640, H = 32, SK = 32768, DH = 192, HD = H * DH, F = 1536, NMOD = 6 * D;
constexpr unsigned SENT = 0xFFFFFFFFu, OOB = 0x7ff00000u;
constexpr int NT = SK / 8;
constexpr int TILEB = 8 * DH * 2;
constexpr int NPS = (NP + NL) / 2;
#define XPF 1
constexpr int NSTAT = NPS * 64 + 2 * 128 + XPF * (NP / 2) * 48;
#define DELAY_M 120
#define DELAY_M2 80
#define DELAY_Q 32
#define POLL_Q 1
#define POLL_M 6
#define PRE_W0 1
#define PRE_SLEEP 2
#define DELAY_B 26
#define DELAY_C 10
#define POLL_C 1
#define XPF_DEPTH 6
constexpr int PW0 = XPF ? 1 : 4;
constexpr int VOL = 16;
constexpr unsigned SPIN_CAP = 1u << 18;
constexpr unsigned W_EPOCH = 0, W_MODG = 4096, W_RDY = W_MODG + 1920 * 8, W_GRDY = 24576, W_QS = 32768, W_ML = 49152, W_CNT = 57344, W_YP = 65536, W_ACT = 90112,
                   W_PART = 131072, W_USED = W_PART + 512 * DH * 4;
constexpr unsigned W_STAMP = 1u << 20;

__device__ __forceinline__ float bf2f(unsigned v) { return __builtin_bit_cast(float, v << 16); }
__device__ __forceinline__ float rbf(float x) { return (float)(__bf16)x; }
__device__ __forceinline__ float lo(unsigned u) { return __builtin_bit_cast(float, u << 16); }
__device__ __forceinline__ float hi(unsigned u) { return __builtin_bit_cast(float, u & 0xffff0000u); }
__device__ __forceinline__ unsigned f2u(float x) { return __builtin_bit_cast(unsigned, x); }
__device__ __forceinline__ float u2f(unsigned x) { return __builtin_bit_cast(float, x); }
__device__ __forceinline__ unsigned pack(float a, float b) { return (f2u(a) >> 16) | (f2u(b) & 0xffff0000u); }
__device__ __forceinline__ unsigned canon(unsigned u) { return u == SENT ? 0x7fc07fc0u : u; }
__device__ __forceinline__ float dot2(unsigned k, unsigned q, float acc) {
  return __builtin_amdgcn_fdot2_f32_bf16(__builtin_bit_cast(bf16x2, k), __builtin_bit_cast(bf16x2, q), acc, false);
}
template <int CTRL> __device__ __forceinline__ float dpp(float x) {
  return u2f(__builtin_amdgcn_mov_dpp(f2u(x), CTRL, 0xf, 0xf, true));
}
constexpr int XOR1 = 0xB1, XOR2 = 0x4E, HMIR = 0x141, RMIR = 0x140, ROR8 = 0x128, SHL4 = 0x104;
__device__ __forceinline__ float silu(float x) { return x / (1.f + __expf(-x)); }
__device__ __forceinline__ float red32(float a, float b) {
  auto r = __builtin_amdgcn_permlane32_swap(f2u(a), f2u(b), false, false);
  return u2f(r[0]) + u2f(r[1]);
}
__device__ __forceinline__ float red16(float a, float b) {
  auto r = __builtin_amdgcn_permlane16_swap(f2u(a), f2u(b), false, false);
  return u2f(r[0]) + u2f(r[1]);
}
__device__ __forceinline__ float red8(float a, float b, bool bit3) {
  const float keep = bit3 ? b : a, send = bit3 ? a : b;
  return keep + dpp<ROR8>(send);
}
__device__ __forceinline__ float max32(float a) {
  auto r = __builtin_amdgcn_permlane32_swap(f2u(a), f2u(a), false, false);
  return fmaxf(u2f(r[0]), u2f(r[1]));
}
__device__ __forceinline__ float max16(float a) {
  auto r = __builtin_amdgcn_permlane16_swap(f2u(a), f2u(a), false, false);
  return fmaxf(u2f(r[0]), u2f(r[1]));
}
__device__ __forceinline__ float wave_sum(float v) {
  v += dpp<XOR1>(v); v += dpp<XOR2>(v); v += dpp<HMIR>(v); v += dpp<RMIR>(v);
  v = red16(v, v); v = red32(v, v);
  return v;
}
__device__ __forceinline__ float red_oct(const float (&a)[8], bool bit3) {
  const float b0 = red32(a[0], a[4]), b1 = red32(a[1], a[5]), b2 = red32(a[2], a[6]), b3 = red32(a[3], a[7]);
  const float c0 = red16(b0, b2), c1 = red16(b1, b3);
  return red8(c0, c1, bit3);
}
__device__ __forceinline__ void mac2(float (&acc)[8], const u32x4& w0, const u32x4& w1, unsigned ap) {
#pragma unroll
  for (int i = 0; i < 4; ++i) {
    acc[2 * i] = dot2(__builtin_amdgcn_perm(w1[i], w0[i], 0x05040100u), ap, acc[2 * i]);
    acc[2 * i + 1] = dot2(__builtin_amdgcn_perm(w1[i], w0[i], 0x07060302u), ap, acc[2 * i + 1]);
  }
}
__device__ __forceinline__ void preperm(unsigned (&wp)[24], const u32x4 (&w)[6]) {
#pragma unroll
  for (int u = 0; u < 3; ++u)
#pragma unroll
    for (int i = 0; i < 4; ++i) {
      wp[u * 8 + 2 * i] = __builtin_amdgcn_perm(w[2 * u + 1][i], w[2 * u][i], 0x05040100u);
      wp[u * 8 + 2 * i + 1] = __builtin_amdgcn_perm(w[2 * u + 1][i], w[2 * u][i], 0x07060302u);
    }
#pragma unroll
  for (int i = 0; i < 24; i += 8)
    asm volatile("" : "+v"(wp[i]), "+v"(wp[i + 1]), "+v"(wp[i + 2]), "+v"(wp[i + 3]), "+v"(wp[i + 4]), "+v"(wp[i + 5]), "+v"(wp[i + 6]), "+v"(wp[i + 7]));
}
__device__ __forceinline__ void macp(float (&acc)[8], const unsigned* wp, unsigned ap) {
#pragma unroll
  for (int i = 0; i < 8; ++i) acc[i] = dot2(wp[i], ap, acc[i]);
}
__device__ __forceinline__ float sum8(const float* p) {
  const float4 a = *(const float4*)p, b = *(const float4*)(p + 4);
  return ((a.x + a.y) + (a.z + a.w)) + ((b.x + b.y) + (b.z + b.w));
}
__device__ __forceinline__ bool valid4(const u32x4& v) { return v[0] != SENT && v[1] != SENT && v[2] != SENT && v[3] != SENT; }
__device__ __forceinline__ unsigned umax3(unsigned a, unsigned b, unsigned c) { return max(max(a, b), c); }
__device__ __forceinline__ unsigned umax4(const u32x4& v) { return umax3(v[0], v[1], max(v[2], v[3])); }
__device__ __forceinline__ u64 ld_gran(const char* ws, unsigned idx) {
  return __hip_atomic_load((const u64*)(ws + W_MODG) + idx, __ATOMIC_RELAXED, __HIP_MEMORY_SCOPE_AGENT);
}
__device__ __forceinline__ unsigned opq(unsigned& v) { asm volatile("" : "+v"(v)); return v; }
#define MEMFENCE() asm volatile("" ::: "memory")
#define WAIT_VM(n) __builtin_amdgcn_s_waitcnt(0x0F70 | ((n) & 15) | (((n) >> 4) << 14))
__device__ __forceinline__ void wg_barrier() {
  asm volatile("s_waitcnt lgkmcnt(0)\n\ts_barrier" ::: "memory");
}
__device__ __forceinline__ void lds_arrive(unsigned* cnt, int lane) {
  asm volatile("s_waitcnt lgkmcnt(0)" ::: "memory");
  if (lane == 0) __hip_atomic_fetch_add(cnt, 1u, __ATOMIC_RELAXED, __HIP_MEMORY_SCOPE_WORKGROUP);
  MEMFENCE();
}
__device__ __forceinline__ void lds_wait(unsigned* cnt, unsigned target) {
  MEMFENCE();
  _Pragma("nounroll") for (unsigned i = 0; i < (1u << 24); ++i) {
    if (__hip_atomic_load(cnt, __ATOMIC_RELAXED, __HIP_MEMORY_SCOPE_WORKGROUP) >= target) break;
    __builtin_amdgcn_s_sleep(1);
  }
  MEMFENCE();
}

struct Tile { u32x4 k[3], v[3]; };
__device__ __forceinline__ void ld_tile(Tile& t, rsrc_t rk, rsrc_t rv, unsigned voff, unsigned soff) {
#pragma unroll
  for (int j = 0; j < 3; ++j) t.k[j] = __builtin_amdgcn_raw_buffer_load_b128(rk, voff + j * 128, soff, KAUX);
#pragma unroll
  for (int j = 0; j < 3; ++j) t.v[j] = __builtin_amdgcn_raw_buffer_load_b128(rv, voff + j * 128, soff, KVAUX);
  MEMFENCE();
  __builtin_amdgcn_sched_barrier(0);
}
__device__ __forceinline__ void ld_tile_lds(unsigned ldst, rsrc_t rk, rsrc_t rv, unsigned v0, unsigned v1, unsigned v2, unsigned soff) {
  unsigned keep;
  asm volatile("s_mov_b32 %0, m0\n\t"
               "s_mov_b32 m0, %1\n\ts_nop 0\n\tbuffer_load_dwordx4 %2, %5, %7 offen " KVMOD " lds\n\t"
               "s_addk_i32 m0, 0x400\n\ts_nop 0\n\tbuffer_load_dwordx4 %3, %5, %7 offen " KVMOD " lds\n\t"
               "s_addk_i32 m0, 0x400\n\ts_nop 0\n\tbuffer_load_dwordx4 %4, %5, %7 offen " KVMOD " lds\n\t"
               "s_addk_i32 m0, 0x400\n\ts_nop 0\n\tbuffer_load_dwordx4 %2, %6, %7 offen " KVMOD " lds\n\t"
               "s_addk_i32 m0, 0x400\n\ts_nop 0\n\tbuffer_load_dwordx4 %3, %6, %7 offen " KVMOD " lds\n\t"
               "s_addk_i32 m0, 0x400\n\ts_nop 0\n\tbuffer_load_dwordx4 %4, %6, %7 offen " KVMOD " lds\n\t"
               "s_mov_b32 m0, %0"
               : "=&s"(keep) : "s"(ldst), "v"(v0), "v"(v1), "v"(v2), "s"(rk), "s"(rv), "s"(soff) : "memory", "scc");
}
__device__ __forceinline__ void use_tile(const Tile& t, const unsigned (&qp)[12], float& m, float& l, float (&acc)[24]) {
  float d = 0.f;
#pragma unroll
  for (int j = 0; j < 3; ++j)
#pragma unroll
    for (int i = 0; i < 4; ++i) d = dot2(t.k[j][i], qp[j * 4 + i], d);
  d += dpp<XOR1>(d); d += dpp<XOR2>(d); d += dpp<HMIR>(d);
  const float sc = rbf(rbf(d) * 0.07216878364870322f);
  const float mn = fmaxf(m, sc);
  const float alpha = __expf(m - mn), p = __expf(sc - mn);
  m = mn; l = l * alpha + p;
#pragma unroll
  for (int j = 0; j < 3; ++j)
#pragma unroll
    for (int i = 0; i < 4; ++i) {
      acc[j * 8 + 2 * i] = fmaf(p, lo(t.v[j][i]), acc[j * 8 + 2 * i] * alpha);
      acc[j * 8 + 2 * i + 1] = fmaf(p, hi(t.v[j][i]), acc[j * 8 + 2 * i + 1] * alpha);
    }
}

#define STAMP(i) do { } while (0)

__global__ void __launch_bounds__(512, 2)
fused_block(const u16* __restrict__ x, const u16* __restrict__ Kc, const u16* __restrict__ Vc, const u16* __restrict__ wq,
            const u16* __restrict__ wo, const u16* __restrict__ wg, const u16* __restrict__ wu, const u16* __restrict__ wd,
            const u16* __restrict__ cvec, const u16* __restrict__ wmod, u16* __restrict__ out, char* ws) {
  __shared__ __attribute__((aligned(16))) unsigned s_flag[8];
  __shared__ __attribute__((aligned(16))) unsigned s_hp[384];
  __shared__ __attribute__((aligned(16))) float s_red[8][32];
  __shared__ __attribute__((aligned(16))) unsigned s_q[192];
  __shared__ __attribute__((aligned(16))) unsigned s_big[(NL > 2 ? NL : 2) * 4 * 1536];
  __shared__ __attribute__((aligned(16))) float s_ml[16][8];
  __shared__ __attribute__((aligned(16))) float s_x1[D];

  const int tid = threadIdx.x, lane = tid & 63, wave = __builtin_amdgcn_readfirstlane(tid >> 6);
  const int b = blockIdx.x, s = b & 7, h = b >> 3;
  const int h0 = h & 15, h1 = h0 + 16, w16 = (h >> 4) * 8 + s;
  float (*s_mrg)[4 * DH] = (float (*)[4 * DH])s_big;
  float (*s_rt)[8] = (float (*)[8])s_red;
  const bool bit3 = (lane & 8) != 0;
  const rsrc_t rw = __builtin_amdgcn_make_buffer_rsrc(ws, 0, W_USED, 0x00020000);
  const rsrc_t rk0 = __builtin_amdgcn_make_buffer_rsrc((char*)Kc + (size_t)h0 * SK * DH * 2, 0, SK * DH * 2, 0x00020000);
  const rsrc_t rv0 = __builtin_amdgcn_make_buffer_rsrc((char*)Vc + (size_t)h0 * SK * DH * 2, 0, SK * DH * 2, 0x00020000);
  const rsrc_t rk1 = __builtin_amdgcn_make_buffer_rsrc((char*)Kc + (size_t)h1 * SK * DH * 2, 0, SK * DH * 2, 0x00020000);
  const rsrc_t rv1 = __builtin_amdgcn_make_buffer_rsrc((char*)Vc + (size_t)h1 * SK * DH * 2, 0, SK * DH * 2, 0x00020000);
  const unsigned voff = (lane >> 3) * 384 + (lane & 7) * 16;
  const bool roleA = s < 5, qprod = s < 6;
  const int ag = (s * 32 + h) / 20, ak = (s * 32 + h) % 20;
  const int bc = (s - 5) * 32 + h;
  const int cc = bc;
  const bool roleC = s >= 5 && cc < 40;
  const int qc = s * 32 + h;
  const int cg = tid & 3, rs = tid >> 2;
  const int pb = w16 * 4 + (wave - 4), gb = NPS * 64 + w16 * 8 + wave;
  auto so0 = [&](int idx) -> unsigned { return (unsigned)(((idx + h0 * STAG) & (NT - 1)) * TILEB); };
  auto so1 = [&](int idx) -> unsigned { return (unsigned)(((idx + h1 * STAG) & (NT - 1)) * TILEB); };

  STAMP(0);
  Tile A, B, P[NP];
  const unsigned ep = __hip_atomic_load((const unsigned*)(ws + W_EPOCH), __ATOMIC_RELAXED, __HIP_MEMORY_SCOPE_AGENT);
  unsigned tag;
  if (wave >= 4) {
    if (wave == 4) {
      const u32x4 s4 = {SENT, SENT, SENT, SENT};
      __builtin_amdgcn_raw_buffer_store_b128(s4, rw, lane < 48 ? W_PART + (h0 * 16 + w16) * (DH * 4) + lane * 16 : OOB, 0, 16);
      __builtin_amdgcn_raw_buffer_store_b128(s4, rw, lane < 48 ? W_PART + (h1 * 16 + w16) * (DH * 4) + lane * 16 : OOB, 0, 16);
      unsigned off = OOB, val = SENT;
      if (lane < 16) { if (qprod) off = W_QS + (qc * 16 + lane) * 4; }
      else if (lane < 48) { if (roleA) off = W_YP + (ag * D + ak * 32 + (lane - 16)) * 4; }
      else if (lane < 56) { if (!roleA) off = W_ACT + (bc * 8 + (lane - 48)) * 4; }
      else if (lane < 60) off = W_ML + ((lane & 2) ? h1 : h0) * 128 + w16 * 8 + (lane & 1) * 4;
      else if (lane == 60) { if (w16 == 0) { off = W_CNT + h0 * 256; val = 0; } }
      __builtin_amdgcn_raw_buffer_store_b32(val, rw, off, 0, 16);
      if (lane < 8) s_flag[lane] = 0;
    }
    wg_barrier();
    tag = ep * 1664525u + 1013904223u;
    if (wave == 4) {
      asm volatile("s_waitcnt vmcnt(0)" ::: "memory");
      if (lane == 0) __hip_atomic_store((u64*)(ws + W_RDY) + b, (u64)tag << 32, __ATOMIC_RELAXED, __HIP_MEMORY_SCOPE_AGENT);
    }
#pragma unroll
    for (int i = 0; i < NP; ++i) {
      if (i & 1) ld_tile(P[i], rk1, rv1, voff, so1(pb + 64 * (i >> 1))); else ld_tile(P[i], rk0, rv0, voff, so0(pb + 64 * (i >> 1)));
      if (i) WAIT_VM(6);
    }
    {
      const unsigned lbase = (unsigned)(uintptr_t)(__attribute__((address_space(3))) unsigned*)s_big + (wave - 4) * (NL * 6144);
#pragma unroll
      for (int i = 0; i < NL; ++i) {
        const int st = pb + 64 * ((NP + i) >> 1);
        if (i & 1) ld_tile_lds(lbase + i * 6144, rk1, rv1, voff, voff + 128, voff + 256, so1(st));
        else ld_tile_lds(lbase + i * 6144, rk0, rv0, voff, voff + 128, voff + 256, so0(st));
        WAIT_VM(6);
      }
    }
    ld_tile(A, rk0, rv0, voff, so0(gb));
    WAIT_VM(6);
    ld_tile(B, rk1, rv1, voff, so1(gb));
    STAMP(1);
  } else {
    const rsrc_t ru = qprod ? __builtin_amdgcn_make_buffer_rsrc((void*)wq, 0, D * HD * 2, 0x00020000)
                            : __builtin_amdgcn_make_buffer_rsrc((void*)wmod, 0, D * NMOD * 2, 0x00020000);
    const unsigned rstride = qprod ? HD * 2 : NMOD * 2;
    const int mu = (s - 6) * 32 + h;
    u32x4 wt[10];
    unsigned av[5];
    {
      const rsrc_t ra = __builtin_amdgcn_make_buffer_rsrc((void*)(qprod ? x : cvec), 0, D * 2, 0x00020000);
      const unsigned a0 = qprod ? lane * 4 : rs * 20, as = qprod ? 256 : 4;
#pragma unroll
      for (int i = 0; i < 5; ++i) av[i] = __builtin_amdgcn_raw_buffer_load_b32(ra, a0 + as * i, 0, 0);
      const unsigned so = qprod ? qc * 64 : mu * 64;
#pragma unroll
      for (int i = 0; i < 10; ++i) wt[i] = __builtin_amdgcn_raw_buffer_load_b128(ru, rs * 10 * rstride + cg * 16, so + i * rstride, WAUX);
    }
    MEMFENCE();
    wg_barrier();
    tag = ep * 1664525u + 1013904223u;
    unsigned arrivals = 0;
    auto gemv = [&](const unsigned (&ap)[5]) -> float {
      float acc[8];
#pragma unroll
      for (int i = 0; i < 8; ++i) acc[i] = 0.f;
#pragma unroll
      for (int u = 0; u < 5; ++u) mac2(acc, wt[2 * u], wt[2 * u + 1], ap[u]);
      float v = red_oct(acc, bit3);
      v += dpp<SHL4>(v);
      float* red = &s_red[(arrivals & 4) + wave][0];
      if ((lane & 4) == 0) red[cg * 8 + (lane >> 3)] = v;
      lds_arrive(&s_flag[1], lane);
      float r = 0.f;
      if (wave == 0) {
        lds_wait(&s_flag[1], arrivals + 4);
        const int cI = lane & 31, o = arrivals & 4;
        r = rbf((s_red[o][cI] + s_red[o + 1][cI]) + (s_red[o + 2][cI] + s_red[o + 3][cI]));
      }
      arrivals += 4;
      return r;
    };
    float n0 = 0.f, n1 = 0.f, n2 = 0.f, n3 = 0.f;
    if (!qprod) {
      unsigned ap[5];
#pragma unroll
      for (int u = 0; u < 5; ++u) ap[u] = pack(rbf(silu(lo(av[u]))), rbf(silu(hi(av[u]))));
#pragma nounroll
      for (int rnd = 0; rnd < 2; ++rnd) {
        const int unit = rnd * 64 + mu;
        const float r = gemv(ap);
        if (rnd == 0) {
          const unsigned so = mu + 64 < 120 ? (mu + 64) * 64 : OOB;
#pragma unroll
          for (int i = 0; i < 10; ++i) wt[i] = __builtin_amdgcn_raw_buffer_load_b128(ru, rs * 10 * rstride + cg * 16, so + i * rstride, WAUX);
        }
        const unsigned pr = pack(r, dpp<XOR1>(r));
        if (wave == 0 && lane < 32 && (lane & 1) == 0 && unit < 120)
          __hip_atomic_store((u64*)(ws + W_MODG) + unit * 16 + (lane >> 1), ((u64)tag << 32) | pr, __ATOMIC_RELAXED, __HIP_MEMORY_SCOPE_AGENT);
        if (rnd == 0) STAMP(1);
      }
    } else {
      float sum = 0.f;
#pragma unroll
      for (int i = 0; i < 5; ++i) sum += lo(av[i]) + hi(av[i]);
      const float mean = wave_sum(sum) * (1.f / D);
      float sq = 0.f;
#pragma unroll
      for (int i = 0; i < 5; ++i) { const float a = lo(av[i]) - mean, c2 = hi(av[i]) - mean; sq += a * a + c2 * c2; }
      const float var = wave_sum(sq) * (1.f / D);
      const float mb = rbf(mean), den = rbf(sqrtf(rbf(rbf(var) + 1e-5f)));
      const unsigned xa = wave == 0 ? av[0] : wave == 1 ? av[1] : wave == 2 ? av[2] : av[3];
      n0 = rbf(rbf(lo(xa) - mb) / den); n1 = rbf(rbf(hi(xa) - mb) / den);
      n2 = rbf(rbf(lo(av[4]) - mb) / den); n3 = rbf(rbf(hi(av[4]) - mb) / den);
      STAMP(1);
    }
    {
      const unsigned i0 = tid, i1 = 320 + tid, i2 = 1920 + tid;
      const unsigned i3 = wave == 0 ? 256 + tid : i0, i4 = wave == 0 ? 576 + tid : i1;
      u64 g0, g1, g2, g3, g4;
      if (qprod) { __builtin_amdgcn_s_sleep(DELAY_M); __builtin_amdgcn_s_sleep(DELAY_M2); }
      _Pragma("nounroll") for (unsigned it = 0; it < SPIN_CAP; ++it) {
        g0 = ld_gran(ws, i0); g1 = ld_gran(ws, i1); g2 = ld_gran(ws, i2); g3 = ld_gran(ws, i3); g4 = ld_gran(ws, i4);
        const bool ok = (unsigned)(g0 >> 32) == tag && (unsigned)(g1 >> 32) == tag && (unsigned)(g2 >> 32) == tag &&
                        (unsigned)(g3 >> 32) == tag && (unsigned)(g4 >> 32) == tag;
        if (__all(ok)) break;
        __builtin_amdgcn_s_sleep(POLL_M);
      }
      const unsigned sa = (unsigned)g0, ca = (unsigned)g1;
      s_hp[tid] = pack(rbf(rbf(n0 * rbf(1.f + lo(ca))) + lo(sa)), rbf(rbf(n1 * rbf(1.f + hi(ca))) + hi(sa)));
      if (wave == 0) {
        const unsigned sa2 = (unsigned)g3, ca2 = (unsigned)g4;
        s_hp[256 + tid] = pack(rbf(rbf(n2 * rbf(1.f + lo(ca2))) + lo(sa2)), rbf(rbf(n3 * rbf(1.f + hi(ca2))) + hi(sa2)));
      }
    }
    STAMP(2);
    lds_arrive(&s_flag[0], lane);
    if (qprod || wave == 0) lds_wait(&s_flag[0], 4);
    if (qprod) {
      unsigned ap[5];
#pragma unroll
      for (int u = 0; u < 5; ++u) ap[u] = s_hp[rs * 5 + u];
      const float r = gemv(ap);
      const unsigned pr = canon(pack(r, dpp<XOR1>(r)));
      if (wave == 0)
        __builtin_amdgcn_raw_buffer_store_b32(pr, rw, (lane < 32 && (lane & 1) == 0) ? W_QS + (qc * 16 + (lane >> 1)) * 4 : OOB, 0, 16);
    }
    STAMP(3);
    if (wave == 0) {
      u32x4 qv;
      __builtin_amdgcn_s_sleep(DELAY_Q);
      _Pragma("nounroll") for (unsigned it = 0; it < SPIN_CAP; ++it) {
        MEMFENCE();
        qv = __builtin_amdgcn_raw_buffer_load_b128(rw, lane < 24 ? W_QS + h0 * 384 + lane * 16 : lane < 48 ? W_QS + h1 * 384 + (lane - 24) * 16 : OOB, 0, 16);
        if (__all(umax4(qv) != SENT)) break;
        __builtin_amdgcn_s_sleep(POLL_Q);
      }
      if (lane < 48) *(u32x4*)&s_q[lane * 4] = qv;
      lds_arrive(&s_flag[2], lane);
    }
    if (wave != 0) {
      const int xb = NPS * 64 + 256 + w16 * 3 + (wave - 1);
#pragma unroll
      for (int i = 0; i < NP; ++i) {
        if (i & 1) ld_tile(P[i], rk1, rv1, voff, so1(xb + 48 * (i >> 1))); else ld_tile(P[i], rk0, rv0, voff, so0(xb + 48 * (i >> 1)));
        if (i) WAIT_VM(XPF_DEPTH);
      }
    }
    ld_tile(A, rk0, rv0, voff, so0(gb));
    if (wave != 0) WAIT_VM(XPF_DEPTH);
    ld_tile(B, rk1, rv1, voff, so1(gb));
  }
  lds_wait(&s_flag[2], 1);
  STAMP(4);
  unsigned q0[12], q1[12];
#pragma unroll
  for (int j = 0; j < 3; ++j) {
    const u32x4 t0 = *(const u32x4*)&s_q[j * 32 + (lane & 7) * 4], t1 = *(const u32x4*)&s_q[96 + j * 32 + (lane & 7) * 4];
#pragma unroll
    for (int i = 0; i < 4; ++i) { q0[j * 4 + i] = t0[i]; q1[j * 4 + i] = t1[i]; }
  }
  float m0 = -INFINITY, l0 = 0.f, acc0[24], m1 = -INFINITY, l1 = 0.f, acc1[24];
#pragma unroll
  for (int i = 0; i < 24; ++i) { acc0[i] = 0.f; acc1[i] = 0.f; }
  const unsigned coff = lane == 0 ? W_CNT + h0 * 256 : OOB;
  unsigned pend = (unsigned)__builtin_amdgcn_raw_ptr_buffer_atomic_add_i32(1, rw, coff, 0, 0);
  if (wave >= PW0) { use_tile(P[0], q0, m0, l0, acc0); use_tile(P[1], q1, m1, l1, acc1); }
  MEMFENCE();
  __builtin_amdgcn_sched_barrier(0);
  use_tile(A, q0, m0, l0, acc0);
  ld_tile(A, rk0, rv0, voff, so0(gb + 128));
  use_tile(B, q1, m1, l1, acc1);
  ld_tile(B, rk1, rv1, voff, so1(gb + 128));
  if (wave >= PW0) {
#pragma unroll
    for (int i = 2; i < NP; ++i) { if (i & 1) use_tile(P[i], q1, m1, l1, acc1); else use_tile(P[i], q0, m0, l0, acc0); }
  }
  if (wave >= 4) {
    const unsigned* lb = s_big + (wave - 4) * (NL * 1536) + lane * 4;
#pragma unroll
    for (int i = 0; i < NL; ++i) {
      Tile T;
#pragma unroll
      for (int j = 0; j < 3; ++j) { T.k[j] = *(const u32x4*)(lb + i * 1536 + j * 256); T.v[j] = *(const u32x4*)(lb + i * 1536 + 768 + j * 256); }
      if (i & 1) use_tile(T, q1, m1, l1, acc1); else use_tile(T, q0, m0, l0, acc0);
    }
  }
  if (wave >= 4) lds_arrive(&s_flag[4], lane);
  u32x4 wa[6], wc[6];
  const int bp0 = rs < 64 ? rs * 3 : 192 + (rs - 64) * 2;
  {
    if (roleA) {
#pragma unroll
      for (int i = 0; i < 6; ++i) wa[i] = ldw<(TNT & 1) != 0>(wo + (size_t)(ag * 768 + rs * 6 + i) * D + ak * 32 + cg * 8);
    } else {
      const u16* wgu = ((cg & 2) ? wu : wg) + (size_t)bp0 * 2 * F + bc * 16 + (cg & 1) * 8;
#pragma unroll
      for (int i = 0; i < 4; ++i) wa[i] = ldw<(TNT & 2) != 0>(wgu + i * F);
      const int ex = wave < 4 ? 4 : 0;
      wa[4] = ldw<(TNT & 2) != 0>(wgu + ex * F);
      wa[5] = ldw<(TNT & 2) != 0>(wgu + (ex + 1) * F);
    }
    if (roleC) {
#pragma unroll
      for (int i = 0; i < 6; ++i) wc[i] = ldw<(TNT & 4) != 0>(wd + (size_t)((tid >> 1) * 6 + i) * D + cc * 16 + (tid & 1) * 8);
    }
    MEMFENCE();
    __builtin_amdgcn_sched_barrier(0);
  }
  STAMP(5);
  while (true) {
    const int tn = NSTAT + __builtin_amdgcn_readfirstlane(pend);
    if (tn >= NT) break;
    const unsigned o0 = so0(tn), o1 = so1(tn);
    use_tile(A, q0, m0, l0, acc0);
    MEMFENCE();
    __builtin_amdgcn_sched_barrier(0);
    pend = (unsigned)__builtin_amdgcn_raw_ptr_buffer_atomic_add_i32(1, rw, coff, 0, 0);
    ld_tile(A, rk0, rv0, voff, o0);
    use_tile(B, q1, m1, l1, acc1);
    ld_tile(B, rk1, rv1, voff, o1);
  }
  lds_wait(&s_flag[4], 4);
  unsigned poff = OOB;
  {
    const int t = tid & 255, hd = (wave >> 2) ? h1 : h0;
    if (t < DH) poff = W_PART + ((hd * 16 + w16) * DH + t) * 4;
    else if (t < DH + 2) poff = W_ML + hd * 128 + w16 * 8 + (t - DH) * 4;
    asm volatile("" : "+v"(poff));
  }
  auto wave_merge = [&](float m, float l, const float (&acc)[24], int slot) {
    float M = fmaxf(m, dpp<ROR8>(m)); M = max16(M); M = max32(M);
    const float w = __expf(m - M);
    l *= w;
    l += dpp<ROR8>(l); l = red16(l, l); l = red32(l, l);
    float r12[12], r6[6];
#pragma unroll
    for (int i = 0; i < 12; ++i) r12[i] = red32(acc[i] * w, acc[i + 12] * w);
#pragma unroll
    for (int i = 0; i < 6; ++i) r6[i] = red16(r12[i], r12[i + 6]);
    const int ebase = 12 * ((lane >> 5) & 1) + 6 * ((lane >> 4) & 1) + 3 * ((lane >> 3) & 1);
#pragma unroll
    for (int i = 0; i < 3; ++i) {
      const float v = red8(r6[i], r6[i + 3], bit3);
      const int e = ebase + i;
      s_mrg[wave][slot * 256 + (e >> 3) * 64 + (lane & 7) * 8 + (e & 7)] = v;
    }
    if (lane == 0) { s_ml[slot][wave] = M; s_mrg[wave][slot * 256 + DH + 1] = l; }
  };
  use_tile(A, q0, m0, l0, acc0);
  wave_merge(m0, l0, acc0, 0);
  MEMFENCE();
  __builtin_amdgcn_sched_barrier(0);
  use_tile(B, q1, m1, l1, acc1);
  STAMP(6);
  wave_merge(m1, l1, acc1, 1);
  wg_barrier();
  {
    const float4 ma = *(const float4*)&s_ml[wave >> 2][0], mb4 = *(const float4*)&s_ml[wave >> 2][4];
    float r[8];
#pragma unroll
    for (int w = 0; w < 8; ++w) r[w] = s_mrg[w][tid];
    __builtin_amdgcn_sched_barrier(0);
    const float M = fmaxf(fmaxf(fmaxf(ma.x, ma.y), fmaxf(ma.z, ma.w)), fmaxf(fmaxf(mb4.x, mb4.y), fmaxf(mb4.z, mb4.w)));
    float o = __expf(ma.x - M) * r[0], o2 = __expf(mb4.x - M) * r[4];
    o = fmaf(__expf(ma.y - M), r[1], o); o2 = fmaf(__expf(mb4.y - M), r[5], o2);
    o = fmaf(__expf(ma.z - M), r[2], o); o2 = fmaf(__expf(mb4.z - M), r[6], o2);
    o = fmaf(__expf(ma.w - M), r[3], o); o2 = fmaf(__expf(mb4.w - M), r[7], o2);
    const float val = (tid & 255) == DH ? M : o + o2;
    __builtin_amdgcn_raw_buffer_store_b32(canon(f2u(val)), rw, poff, 0, 16);
  }
  STAMP(7);
  if (roleA) {
    unsigned wp[24];
    preperm(wp, wa);
    unsigned yoff = lane < 32 ? W_YP + (ag * D + ak * 32 + lane) * 4 : OOB;
    asm volatile("" : "+v"(yoff));
    wg_barrier();
    {
      unsigned po[3], bi[3];
#pragma unroll
      for (int i = 0; i < 3; ++i) {
        const int qd = lane + 64 * i, hh = qd / 48;
        po[i] = W_PART + (((ag * 4 + hh) * 16 + wave) * DH + (qd - hh * 48) * 4) * 4;
        bi[i] = hh * 16 + wave;
      }
      unsigned mo = W_ML + ag * 512 + lane * 8;
      u32x4 a[6]; u32x2 ml = {0u, 0u};
      bool pv = false, mv = wave != 0;
      _Pragma("nounroll") for (unsigned it = 0; it < SPIN_CAP; ++it) {
        MEMFENCE();
        if (!mv) ml = __builtin_amdgcn_raw_buffer_load_b64(rw, opq(mo), 0, VOL);
        if (!pv) {
          opq(po[0]); opq(po[1]); opq(po[2]);
#pragma unroll
          for (int i = 0; i < 6; ++i) a[i] = __builtin_amdgcn_raw_buffer_load_b128(rw, po[i % 3], (i / 3) * (8 * DH * 4), VOL);
          pv = __all(umax3(umax3(umax4(a[0]), umax4(a[1]), umax4(a[2])), umax4(a[3]), max(umax4(a[4]), umax4(a[5]))) != SENT);
        }
        mv = __all(max(ml[0], ml[1]) != SENT);
        if (pv && mv) break;
        __builtin_amdgcn_s_sleep(POLL_A);
      }
      if (wave == 0) {
        if (ak == 0 && lane == 0)
          __hip_atomic_store((u64*)(ws + W_GRDY + ag * 128), (u64)tag << 32, __ATOMIC_RELAXED, __HIP_MEMORY_SCOPE_AGENT);
        const float mi = u2f(ml[0]);
        float M = fmaxf(mi, dpp<XOR1>(mi)); M = fmaxf(M, dpp<XOR2>(M)); M = fmaxf(M, dpp<HMIR>(M)); M = fmaxf(M, dpp<RMIR>(M));
        const float e = __expf(mi - M);
        float L = e * u2f(ml[1]);
        L += dpp<XOR1>(L); L += dpp<XOR2>(L); L += dpp<HMIR>(L); L += dpp<RMIR>(L);
        s_x1[lane] = e * __builtin_amdgcn_rcpf(L);
        lds_arrive(&s_flag[5], lane);
      } else {
        lds_wait(&s_flag[5], 1);
      }
      STAMP(8);
#pragma unroll
      for (int i = 0; i < 3; ++i) {
        const float f0 = s_x1[bi[i]], f1 = s_x1[bi[i] + 8];
        float4 t;
        t.x = fmaf(u2f(a[i + 3][0]), f1, u2f(a[i][0]) * f0); t.y = fmaf(u2f(a[i + 3][1]), f1, u2f(a[i][1]) * f0);
        t.z = fmaf(u2f(a[i + 3][2]), f1, u2f(a[i][2]) * f0); t.w = fmaf(u2f(a[i + 3][3]), f1, u2f(a[i][3]) * f0);
        *(float4*)&s_mrg[wave][i * 256 + lane * 4] = t;
      }
    }
    wg_barrier();
    if (tid < 384) {
      float2 r[8];
#pragma unroll
      for (int i = 0; i < 8; ++i) r[i] = *(const float2*)&s_mrg[i][tid * 2];
      const float o0 = ((r[0].x + r[1].x) + (r[2].x + r[3].x)) + ((r[4].x + r[5].x) + (r[6].x + r[7].x));
      const float o1 = ((r[0].y + r[1].y) + (r[2].y + r[3].y)) + ((r[4].y + r[5].y) + (r[6].y + r[7].y));
      s_hp[tid] = pack(rbf(o0), rbf(o1));
    }
    wg_barrier();
    float ac[8];
#pragma unroll
    for (int i = 0; i < 8; ++i) ac[i] = 0.f;
#pragma unroll
    for (int u = 0; u < 3; ++u) macp(ac, wp + 8 * u, s_hp[rs * 3 + u]);
    float v = red_oct(ac, bit3);
    v += dpp<SHL4>(v);
    if ((lane & 4) == 0) s_rt[cg * 8 + (lane >> 3)][wave] = v;
    wg_barrier();
    if (wave == 0) {
      const float y = sum8(&s_rt[lane & 31][0]);
      __builtin_amdgcn_raw_buffer_store_b32(canon(f2u(y)), rw, yoff, 0, 16);
    }
    STAMP(9);
  } else {
    const int t2 = tid < 320 ? tid : 0;
    unsigned wp[24], wq2[24];
    preperm(wp, wa);
    if (roleC) preperm(wq2, wc);
    unsigned gga, gsb, gcb, ggb;
    const unsigned xpair = ((const unsigned*)x)[t2];
    {
      u64 g0, g1, g2, g3;
      const unsigned i3 = 1600 + (roleC ? cc * 8 : 0) + ((lane & 15) >> 1);
      _Pragma("nounroll") for (unsigned it = 0; it < SPIN_CAP; ++it) {
        g0 = ld_gran(ws, 640 + t2); g1 = ld_gran(ws, 960 + t2); g2 = ld_gran(ws, 1280 + t2); g3 = ld_gran(ws, i3);
        if (__all((unsigned)(g0 >> 32) == tag && (unsigned)(g1 >> 32) == tag && (unsigned)(g2 >> 32) == tag && (unsigned)(g3 >> 32) == tag)) break;
        __builtin_amdgcn_s_sleep(2);
      }
      gga = (unsigned)g0; gsb = (unsigned)g1; gcb = (unsigned)g2; ggb = (unsigned)g3;
    }
    float c1a = rbf(1.f + lo(gcb)), c1b = rbf(1.f + hi(gcb));
    unsigned aoff = (lane < 16 && (lane & 1) == 0) ? W_ACT + (bc * 8 + (lane >> 1)) * 4 : OOB;
    asm volatile("" : "+v"(c1a), "+v"(c1b), "+v"(aoff));
    float xa = 0.f, xb = 0.f;
    u32x2 ya[8];
    if (wave < 5) {
      if (wave == 0 || !PRE_W0) {
        _Pragma("nounroll") for (unsigned it = 0; it < SPIN_CAP; ++it) {
          MEMFENCE();
          const u64 gr = __hip_atomic_load((const u64*)(ws + W_GRDY + (lane & 7) * 128), __ATOMIC_RELAXED, __HIP_MEMORY_SCOPE_AGENT);
          if (__all((unsigned)(gr >> 32) == tag)) break;
          __builtin_amdgcn_s_sleep(PRE_SLEEP);
        }
        if (PRE_W0) lds_arrive(&s_flag[5], lane);
      } else {
        lds_wait(&s_flag[5], 1);
      }
      __builtin_amdgcn_s_sleep(DELAY_B);
      float y0 = 0.f, y1 = 0.f;
      unsigned yo = W_YP + t2 * 8;
      auto y_issue = [&](u32x2 (&d)[8]) {
        opq(yo);
#pragma unroll
        for (int i = 0; i < 8; ++i) d[i] = __builtin_amdgcn_raw_buffer_load_b64(rw, yo, i * (D * 4), VOL);
        MEMFENCE();
      };
      auto y_take = [&](const u32x2 (&d)[8]) -> bool {
        unsigned mx = 0;
        y0 = 0.f; y1 = 0.f;
#pragma unroll
        for (int i = 0; i < 8; ++i) { mx = umax3(mx, d[i][0], d[i][1]); y0 += u2f(d[i][0]); y1 += u2f(d[i][1]); }
        return __all(mx != SENT);
      };
      _Pragma("nounroll") for (unsigned it = 0; it < SPIN_CAP; ++it) {
        y_issue(ya);
        if (y_take(ya)) break;
        __builtin_amdgcn_s_sleep(POLL_B);
      }
      xa = rbf(lo(xpair) + rbf(lo(gga) * rbf(y0)));
      xb = rbf(hi(xpair) + rbf(hi(gga) * rbf(y1)));
      const float k = u2f(__builtin_amdgcn_readfirstlane(f2u(xa)));
      const float da = xa - k, db = xb - k;
      const float S = wave_sum(da + db), Q = wave_sum(fmaf(da, da, db * db));
      *(float2*)&s_x1[tid * 2] = make_float2(xa, xb);
      if (lane == 0) *(float2*)&s_ml[4][wave * 2] = make_float2(k + S * (1.f / 128), Q - S * S * (1.f / 128));
    }
    STAMP(8);
    wg_barrier();
    if (wave < 5) {
      const float4 s0 = *(const float4*)&s_ml[4][0], s1 = *(const float4*)&s_ml[4][4];
      const float2 s2 = *(const float2*)&s_ml[5][0];
      __builtin_amdgcn_sched_barrier(0);
      const float mean = (((s0.x + s0.z) + (s1.x + s1.z)) + s2.x) * 0.2f;
      const float d0 = s0.x - mean, d1 = s0.z - mean, d2 = s1.x - mean, d3 = s1.z - mean, d4 = s2.x - mean;
      const float M2 = fmaf(128.f, fmaf(d0, d0, d1 * d1) + fmaf(d2, d2, fmaf(d3, d3, d4 * d4)), ((s0.y + s0.w) + (s1.y + s1.w)) + s2.y);
      const float mb = rbf(mean), den = rbf(__builtin_amdgcn_sqrtf(rbf(rbf(M2 * (1.f / D)) + 1e-5f)));
      const float rden = __builtin_amdgcn_rcpf(den);
      const float na = rbf(rbf(xa - mb) * rden), nb = rbf(rbf(xb - mb) * rden);
      s_hp[tid] = pack(rbf(rbf(na * c1a) + lo(gsb)), rbf(rbf(nb * c1b) + hi(gsb)));
    }
    wg_barrier();
    float ac[8];
#pragma unroll
    for (int i = 0; i < 8; ++i) ac[i] = 0.f;
    macp(ac, wp, s_hp[bp0]);
    macp(ac, wp + 8, s_hp[bp0 + 1]);
    if (wave < 4) macp(ac, wp + 16, s_hp[bp0 + 2]);
    float v = red_oct(ac, bit3);
    v += dpp<SHL4>(v);
    if ((lane & 4) == 0) s_rt[cg * 8 + (lane >> 3)][wave] = v;
    wg_barrier();
    if (wave == 0) {
      const unsigned gu = f2u(rbf(sum8(&s_rt[lane & 31][0])));
      const auto sw = __builtin_amdgcn_permlane16_swap(gu, gu, false, false);
      const float g = u2f(sw[0]), u = u2f(sw[1]);
      const float av = rbf(rbf(g * __builtin_amdgcn_rcpf(1.f + __expf(-g))) * u);
      const unsigned pr = canon(pack(av, dpp<XOR1>(av)));
      __builtin_amdgcn_raw_buffer_store_b32(pr, rw, aoff, 0, 16);
    }
    STAMP(9);
    if (roleC) {
      u32x3 ap;
      float x1v = s_x1[cc * 16 + (lane & 15)], gbv = (lane & 1) ? hi(ggb) : lo(ggb);
      u16* op = out + cc * 16 + (lane & 15);
      asm volatile("" : "+v"(x1v), "+v"(gbv), "+v"(op));
      __builtin_amdgcn_s_sleep(DELAY_C);
      _Pragma("nounroll") for (unsigned it = 0; it < SPIN_CAP; ++it) {
        MEMFENCE();
        ap = __builtin_amdgcn_raw_buffer_load_b96(rw, W_ACT + (tid >> 1) * 12, 0, 16);
        if (__all(umax3(ap[0], ap[1], ap[2]) != SENT)) break;
        __builtin_amdgcn_s_sleep(POLL_C);
      }
      STAMP(10);
      float dc[8];
#pragma unroll
      for (int i = 0; i < 8; ++i) dc[i] = 0.f;
#pragma unroll
      for (int u = 0; u < 3; ++u) macp(dc, wq2 + 8 * u, ap[u]);
      float dv = red_oct(dc, bit3);
      dv += dpp<SHL4>(dv); dv += dpp<XOR2>(dv);
      if ((lane & 6) == 0) s_ml[(lane & 1) * 8 + (lane >> 3)][wave] = dv;
      wg_barrier();
      if (tid < 16) {
        const float dn = sum8(&s_ml[tid][0]);
        const float r = x1v + rbf(gbv * rbf(dn));
        *op = (u16)(f2u(rbf(r)) >> 16);
        if (cc == 0 && tid == 0) __hip_atomic_store((unsigned*)(ws + W_EPOCH), tag, __ATOMIC_RELAXED, __HIP_MEMORY_SCOPE_AGENT);
      }
      STAMP(11);
    }
  }
}

extern "C" void kernel_launch(void* const* d_in, const int* in_sizes, int n_in, void* d_out, int out_size, void* d_ws, size_t ws_size,
                              hipStream_t stream) {
  fused_block<<<256, 512, 0, stream>>>((const u16*)d_in[0], (const u16*)d_in[1], (const u16*)d_in[2], (const u16*)d_in[3],
                                       (const u16*)d_in[4], (const u16*)d_in[6], (const u16*)d_in[7], (const u16*)d_in[8],
                                       (const u16*)d_in[9], (const u16*)d_in[10], (u16*)d_out, (char*)d_ws);
}
```
